```python
import torch

def _softmax(x, axis=-1):
    return torch.softmax(x, dim=axis)
def _gelu(x):
    return torch.nn.functional.gelu(x, approximate="tanh")
def _silu(x):
    return torch.nn.functional.silu(x)
def _sigmoid(x):
    return torch.sigmoid(x)
def _layernorm(x, axis=-1, eps=1e-5):
    m = x.mean(dim=axis, keepdim=True)
    v = x.var(dim=axis, keepdim=True, unbiased=False)
    return (x - m) / torch.sqrt(v + eps)
def _scatter_add(tgt, idx, src, axis=0):
    return tgt.clone().index_add_(axis, idx, src)
def _segment_sum(src, seg, n):
    out = torch.zeros((n,) + src.shape[1:], dtype=src.dtype)
    return out.index_add_(0, seg, src)
def _pad(x, pw):
    flat = tuple(v for pair in reversed(pw) for v in pair)
    return torch.nn.functional.pad(x, flat)

kernel_name = 'standalone__glu_gated_reglu__prompt'

def setup_inputs():

    _w = (lambda a: (lambda t: t.to(torch.bfloat16) if t.is_floating_point() else t)(torch.as_tensor(a)))
    B, S, D, F = 1, 2048, 512, 1792
    return {
        "x": _w((lambda *s: torch.randn(*s, dtype=torch.bfloat16) * 0.05)(B, S, D)),
        "W_gate": _w((lambda *s: torch.randn(*s, dtype=torch.bfloat16) * 0.05)(D, F) * (2.0 / D) ** 0.5),
        "W_up": _w((lambda *s: torch.randn(*s, dtype=torch.bfloat16) * 0.05)(D, F) * (2.0 / D) ** 0.5),
        "W_down": _w((lambda *s: torch.randn(*s, dtype=torch.bfloat16) * 0.05)(F, D) * (1.0 / F) ** 0.5),
    }

def reference(x, W_gate, W_up, W_down):
    gate = torch.einsum("bsd,df->bsf", x, W_gate)
    up = torch.einsum("bsd,df->bsf", x, W_up)
    gate = gate * (lambda x, dt: x.to(dt))(gate > 0, gate.dtype)
    h = gate * up
    return torch.einsum("bsf,fd->bsd", h, W_down)
```

```cpp
#include <hip/hip_runtime.h>
#include <hip/hip_bf16.h>
typedef short bf16x8 __attribute__((ext_vector_type(8)));
typedef float f32x4 __attribute__((ext_vector_type(4)));
constexpr int S=2048, D=512, F=1792;
__device__ __forceinline__ float bf2f(unsigned short b){ return __builtin_bit_cast(float,(unsigned)b<<16);}
__device__ __forceinline__ unsigned short f2bf(float f){ __hip_bfloat16 h=__float2bfloat16(f); return __builtin_bit_cast(unsigned short,h);}
__global__ __launch_bounds__(256) void k1(const unsigned short* x,const unsigned short* wg,const unsigned short* wu,unsigned short* h){
  int wid=(blockIdx.x*256+threadIdx.x)>>6, lane=threadIdx.x&63;
  int mt=wid/(F/16), ft=wid%(F/16);
  f32x4 g={0,0,0,0},u={0,0,0,0};
  int r=lane&15,q=lane>>4;
  for(int k=0;k<D;k+=32){
    bf16x8 a=*(const bf16x8*)(x+(mt*16+r)*D+k+q*8);
    bf16x8 bg,bu;
    for(int j=0;j<8;j++){ bg[j]=wg[(k+q*8+j)*F+ft*16+r]; bu[j]=wu[(k+q*8+j)*F+ft*16+r]; }
    g=__builtin_amdgcn_mfma_f32_16x16x32_bf16(a,bg,g,0,0,0);
    u=__builtin_amdgcn_mfma_f32_16x16x32_bf16(a,bu,u,0,0,0);
  }
  for(int j=0;j<4;j++){
    float gg=bf2f(f2bf(g[j])), uu=bf2f(f2bf(u[j]));
    gg = gg*(gg>0.f?1.f:0.f);
    h[(mt*16+q*4+j)*F+ft*16+r]=f2bf(gg*uu);
  }
}
__global__ __launch_bounds__(256) void k2(const unsigned short* h,const unsigned short* wd,unsigned short* out){
  int wid=(blockIdx.x*256+threadIdx.x)>>6, lane=threadIdx.x&63;
  int mt=wid/(D/16), nt=wid%(D/16);
  f32x4 c={0,0,0,0};
  int r=lane&15,q=lane>>4;
  for(int k=0;k<F;k+=32){
    bf16x8 a=*(const bf16x8*)(h+(mt*16+r)*F+k+q*8);
    bf16x8 b;
    for(int j=0;j<8;j++){ b[j]=wd[(k+q*8+j)*D+nt*16+r]; }
    c=__builtin_amdgcn_mfma_f32_16x16x32_bf16(a,b,c,0,0,0);
  }
  for(int j=0;j<4;j++) out[(mt*16+q*4+j)*D+nt*16+r]=f2bf(c[j]);
}
extern "C" void kernel_launch(void* const* d_in, const int* in_sizes, int n_in, void* d_out, int out_size, void* d_ws, size_t ws_size, hipStream_t stream){
  const unsigned short* x=(const unsigned short*)d_in[0];
  const unsigned short* wg=(const unsigned short*)d_in[1];
  const unsigned short* wu=(const unsigned short*)d_in[2];
  const unsigned short* wd=(const unsigned short*)d_in[3];
  unsigned short* h=(unsigned short*)d_ws;
  k1<<<(S/16)*(F/16)/4,256,0,stream>>>(x,wg,wu,h);
  k2<<<(S/16)*(D/16)/4,256,0,stream>>>(h,wd,(unsigned short*)d_out);
}
```

```cpp
#include <hip/hip_runtime.h>
#include <hip/hip_bf16.h>
#define LAS __attribute__((address_space(3)))
#define GAS __attribute__((address_space(1)))
typedef short s16x4 __attribute__((ext_vector_type(4)));
typedef short bf16x8 __attribute__((ext_vector_type(8)));
typedef float f32x16 __attribute__((ext_vector_type(16)));
typedef float f32x4 __attribute__((ext_vector_type(4)));
typedef float f32x2 __attribute__((ext_vector_type(2)));
typedef __bf16 bf16x2 __attribute__((ext_vector_type(2)));
typedef unsigned u32x4 __attribute__((ext_vector_type(4)));
typedef short s16x2 __attribute__((ext_vector_type(2)));
#define DI __device__ __forceinline__
constexpr int S_=2048, D_=512, F_=1792;
constexpr int RSRC3=0x00020000;
#define DIAG 0
#define STAMP_OFF (128u<<20)
#define STAMP(k)
#define WAITV(n) asm volatile("s_waitcnt vmcnt(" #n ")":::"memory")
#define WAITL(n) asm volatile("s_waitcnt lgkmcnt(" #n ")":::"memory")
#define BAR() __builtin_amdgcn_s_barrier()
#define SCHED() __builtin_amdgcn_sched_barrier(0)

constexpr int X_OFF=0, G_OFF=16384, U_OFF=32768+32, STG=49408, NSTG=3;
constexpr int LDS1=STG*NSTG;

DI unsigned pk_bf16(float a,float b){ f32x2 v={a,b}; bf16x2 r=__builtin_convertvector(v,bf16x2); return __builtin_bit_cast(unsigned,r);}
DI bf16x8 trpair(LAS char* p,int off0,int off1){
  s16x4 a=__builtin_amdgcn_ds_read_tr16_b64_v4i16((LAS s16x4*)(p+off0));
  s16x4 b=__builtin_amdgcn_ds_read_tr16_b64_v4i16((LAS s16x4*)(p+off1));
  return __builtin_shufflevector(a,b,0,1,2,3,4,5,6,7);
}
DI void dma(__amdgpu_buffer_rsrc_t rs,LAS char* dst,int voff,int soff){
  __builtin_amdgcn_raw_ptr_buffer_load_lds(rs,(LAS void*)dst,16,voff,soff,0,0);
}

__global__ __launch_bounds__(512,2) void k_gateup(const void* x,const void* wg,const void* wu,void* ws){
  __shared__ __attribute__((aligned(256))) char smem_[LDS1];
  LAS char* smem=(LAS char*)smem_;
  const int b=blockIdx.x, xcd=b&7, idx=b>>3;
  STAMP(0);
  if(idx>=28) return;
  const int mb=(xcd&3)*4+(idx&3), fb=(xcd>>2)*7+(idx>>2);
  const int lane=threadIdx.x&63, w=__builtin_amdgcn_readfirstlane(threadIdx.x>>6);
  if(w>=4){
    const int lw=w-4;
    auto rx=__builtin_amdgcn_make_buffer_rsrc((void*)x,0,S_*D_*2,RSRC3);
    auto rg=__builtin_amdgcn_make_buffer_rsrc((void*)wg,0,D_*F_*2,RSRC3);
    auto ru=__builtin_amdgcn_make_buffer_rsrc((void*)wu,0,D_*F_*2,RSRC3);
    const int xr=(mb*128+32*lw+(lane>>3))*1024;
    const int vx0=xr+(((lane&7)^((lane>>4)))<<4);
    const int vx1=xr+8*1024+(((lane&7)^((lane>>4)+4))<<4);
    const int vw=(16*lw+(lane>>4))*(F_*2)+fb*256+(((lane&15)^((lane>>4)<<2))<<4);
    auto issue=[&](int kt,int slot){
      LAS char* sb=smem+slot*STG;
      dma(rx,sb+X_OFF+lw*4096+0,   vx0,kt*128);
      dma(rx,sb+X_OFF+lw*4096+1024,vx1,kt*128);
      dma(rx,sb+X_OFF+lw*4096+2048,vx0,kt*128+16*1024);
      dma(rx,sb+X_OFF+lw*4096+3072,vx1,kt*128+16*1024);
      #pragma unroll
      for(int i=0;i<4;i++){
        dma(rg,sb+G_OFF+lw*4096+i*1024,vw,(kt*64+4*i)*(F_*2));
        dma(ru,sb+U_OFF+lw*4096+i*1024,vw,(kt*64+4*i)*(F_*2));
      }
    };
    issue(0,0); issue(1,1); issue(2,2);
    WAITV(24); BAR();
    WAITV(12); BAR();
    int slot=0;
    for(int t=2;t<8;t++){
      if(t+1<8){ issue(t+1,slot); WAITV(12);} else { WAITV(0);}
      slot=slot==2?0:slot+1;
      BAR();
    }
    return;
  }
  const int wr=w>>1, wc=w&1;
  const int h=lane>>5, isU=(lane>>4)&1, q=(lane&15)>>2, p=lane&3;
  int aw[2], ax[4];
  #pragma unroll
  for(int ui=0;ui<2;ui++) aw[ui]=(isU?U_OFF:G_OFF)+256*(8*h+q)+64*((2*wc+ui)^q)+8*p;
  { const int row=64*wr+(lane&31);
    #pragma unroll
    for(int s=0;s<4;s++) ax[s]=X_OFF+128*row+16*((2*s+h)^((row>>1)&7)); }
  f32x16 acc[4][2];
  #pragma unroll
  for(int c=0;c<4;c++) for(int m=0;m<2;m++) for(int i=0;i<16;i++) acc[c][m][i]=0.f;
  bf16x8 fa[2][4], fbx[2][2];
  auto rd=[&](int buf,LAS char* sb,int s){
    #pragma unroll
    for(int m=0;m<2;m++) fbx[buf][m]=*(LAS bf16x8*)(sb+ax[s]+m*4096);
    #pragma unroll
    for(int c=0;c<4;c++) fa[buf][c]=trpair(sb+aw[c>>1],256*(16*s)+32*(c&1),256*(16*s+4)+32*(c&1));
  };
  auto mm=[&](int buf){
    #pragma unroll
    for(int c=0;c<4;c++)
      #pragma unroll
      for(int m=0;m<2;m++) acc[c][m]=__builtin_amdgcn_mfma_f32_32x32x16_bf16(fa[buf][c],fbx[buf][m],acc[c][m],0,0,0);
  };
  auto shape=[&](){
    #pragma unroll
    for(int i=0;i<5;i++){ __builtin_amdgcn_sched_group_barrier(0x8,1,0); __builtin_amdgcn_sched_group_barrier(0x100,2,0);}
    __builtin_amdgcn_sched_group_barrier(0x8,3,0);
  };
  BAR();
  STAMP(1);
  int slot=0;
  rd(0,smem,0);
  for(int t=0;t<8;t++){
    LAS char* sb=smem+slot*STG;
    SCHED();
    rd(1,sb,1); mm(0); shape(); SCHED();
    rd(0,sb,2); mm(1); shape(); SCHED();
    rd(1,sb,3); mm(0); shape(); SCHED();
    slot=slot==2?0:slot+1;
    if(t<7){
      WAITL(0); BAR(); SCHED();
      rd(0,smem+slot*STG,0); mm(1); shape(); SCHED();
    } else mm(1);
  }
  STAMP(2);
  auto rh=__builtin_amdgcn_make_buffer_rsrc(ws,0,S_*F_*2,RSRC3);
  #pragma unroll
  for(int m=0;m<2;m++)
    #pragma unroll
    for(int c=0;c<4;c++){
      u32x4 o;
      #pragma unroll
      for(int e=0;e<4;e++){
        unsigned gp=pk_bf16(acc[c][m][2*e],acc[c][m][2*e+1]);
        unsigned up=pk_bf16(acc[c][m][8+2*e],acc[c][m][8+2*e+1]);
        s16x2 z={0,0};
        gp=__builtin_bit_cast(unsigned,__builtin_elementwise_max(__builtin_bit_cast(s16x2,gp),z));
        float g0=__builtin_bit_cast(float,gp<<16), g1=__builtin_bit_cast(float,gp&0xffff0000u);
        float u0=__builtin_bit_cast(float,up<<16), u1=__builtin_bit_cast(float,up&0xffff0000u);
        o[e]=pk_bf16(g0*u0,g1*u1);
      }
      const int MT=mb*4+wr*2+m, KS=fb*8+wc*4+c;
      __builtin_amdgcn_raw_buffer_store_b128(o,rh,(MT*112+KS)*1024+lane*16,0,0);
    }
  STAMP(3);
}

constexpr int QREG=40960;
__global__ __launch_bounds__(512,2) void k_down(const void* wd,void* ws,void* out){
  __shared__ __attribute__((aligned(256))) char smem_[163840];
  LAS char* smem=(LAS char*)smem_;
  const int b=blockIdx.x, xcd=b&7, idx=b>>3;
  STAMP(4);
  const int mi=xcd*4+(idx&3), ni=idx>>2;
  const int lane=threadIdx.x&63, w=__builtin_amdgcn_readfirstlane(threadIdx.x>>6);
  const int kq=w&3, mh=w>>2;
  auto rd_=__builtin_amdgcn_make_buffer_rsrc((void*)wd,0,F_*D_*2,RSRC3);
  auto rh=__builtin_amdgcn_make_buffer_rsrc(ws,0,S_*F_*2,RSRC3);
  LAS char* qb=smem+kq*QREG;
  const int vd=(kq*448+(lane>>3))*1024+ni*128+(((lane&7)^(((lane>>4)&1)<<2))<<4);
  #pragma unroll
  for(int i=0;i<12;i++) dma(rd_,qb+(mh*12+i)*1024,vd,(mh*12+i)*8*1024);
  #pragma unroll
  for(int i=0;i<8;i++) dma(rd_,qb+24576+(mh*8+i)*1024,vd,(24+mh*8+i)*8*1024);
  u32x4 hf[28];
  const int MT=mi*2+mh;
  #pragma unroll
  for(int i=0;i<28;i++) hf[i]=__builtin_amdgcn_raw_buffer_load_b128(rh,lane*16,(MT*112+kq*28+i)*1024,0);
  SCHED();
  const int h=lane>>5, blk=(lane>>4)&1, q=(lane&15)>>2, p=lane&3;
  int ad[2];
  #pragma unroll
  for(int nt=0;nt<2;nt++) ad[nt]=128*(4*h+q)+64*(nt^(q>>1))+32*blk+8*p;
  WAITV(36); BAR();
  bf16x8 wa[12][2];
  #pragma unroll
  for(int ks=0;ks<12;ks++)
    #pragma unroll
    for(int nt=0;nt<2;nt++) wa[ks][nt]=trpair(qb+ad[nt],128*16*ks,128*(16*ks+8));
  WAITL(0); SCHED(); BAR();
  #pragma unroll
  for(int i=0;i<8;i++) dma(rd_,qb+(mh*8+i)*1024,vd,(40+mh*8+i)*8*1024);
  f32x16 acc[2];
  for(int i=0;i<16;i++){acc[0][i]=0.f;acc[1][i]=0.f;}
  #pragma unroll
  for(int ks=0;ks<12;ks++)
    #pragma unroll
    for(int nt=0;nt<2;nt++) acc[nt]=__builtin_amdgcn_mfma_f32_32x32x16_bf16(wa[ks][nt],__builtin_bit_cast(bf16x8,hf[ks]),acc[nt],0,0,0);
  WAITV(0); BAR();
  #pragma unroll
  for(int ks=12;ks<28;ks++){
    const int base=ks<20?24576+(ks-12)*2048:(ks-20)*2048;
    #pragma unroll
    for(int nt=0;nt<2;nt++){ bf16x8 a=trpair(qb+ad[nt],base,base+1024);
      acc[nt]=__builtin_amdgcn_mfma_f32_32x32x16_bf16(a,__builtin_bit_cast(bf16x8,hf[ks]),acc[nt],0,0,0);}
  }
  WAITL(0); BAR();
  STAMP(5);
  { LAS char* pw=smem+kq*(64*272)+(mh*32+(lane&31))*272+16*h;
    #pragma unroll
    for(int nt=0;nt<2;nt++)
      #pragma unroll
      for(int g=0;g<4;g++){ f32x4 v={acc[nt][4*g],acc[nt][4*g+1],acc[nt][4*g+2],acc[nt][4*g+3]}; *(LAS f32x4*)(pw+nt*128+g*32)=v; }
  }
  WAITL(0); BAR();
  { const int row=8*w+(lane>>3); LAS char* pr=smem+row*272+(lane&7)*32;
    f32x4 a={0,0,0,0},c={0,0,0,0};
    #pragma unroll
    for(int k=0;k<4;k++){ a+=*(LAS f32x4*)(pr+k*64*272); c+=*(LAS f32x4*)(pr+k*64*272+16);}
    u32x4 o={pk_bf16(a[0],a[1]),pk_bf16(a[2],a[3]),pk_bf16(c[0],c[1]),pk_bf16(c[2],c[3])};
    *(u32x4*)((char*)out+(size_t)(mi*64+row)*1024+ni*128+(lane&7)*16)=o;
  }
  STAMP(6);
}

extern "C" void kernel_launch(void* const* d_in,const int* in_sizes,int n_in,void* d_out,int out_size,void* d_ws,size_t ws_size,hipStream_t stream){
  k_gateup<<<256,512,0,stream>>>(d_in[0],d_in[1],d_in[2],d_ws);
  k_down<<<256,512,0,stream>>>(d_in[3],d_ws,d_out);
}
```

```cpp
#include <hip/hip_runtime.h>
#include <hip/hip_bf16.h>
#define LAS __attribute__((address_space(3)))
#define GAS __attribute__((address_space(1)))
typedef short s16x4 __attribute__((ext_vector_type(4)));
typedef short bf16x8 __attribute__((ext_vector_type(8)));
typedef float f32x16 __attribute__((ext_vector_type(16)));
typedef float f32x4 __attribute__((ext_vector_type(4)));
typedef float f32x2 __attribute__((ext_vector_type(2)));
typedef __bf16 bf16x2 __attribute__((ext_vector_type(2)));
typedef unsigned u32x4 __attribute__((ext_vector_type(4)));
typedef short s16x2 __attribute__((ext_vector_type(2)));
#define DI __device__ __forceinline__
constexpr int S_=2048, D_=512, F_=1792;
constexpr int RSRC3=0x00020000;
#define DIAG 0
#define STAMP_OFF (128u<<20)
#define STAMP(k)
#define PAD1 0
#define PAD2 0
#define PADBEG() unsigned long long pad0_=__builtin_amdgcn_s_memrealtime()
#define PADEND(T) do{ if(T){ asm volatile("s_waitcnt vmcnt(0)":::"memory"); while(__builtin_amdgcn_s_memrealtime()-pad0_<(T)) __builtin_amdgcn_s_sleep(1);} }while(0)
#define WAITV(n) asm volatile("s_waitcnt vmcnt(" #n ")":::"memory")
#define WAITL(n) asm volatile("s_waitcnt lgkmcnt(" #n ")":::"memory")
#define BAR() __builtin_amdgcn_s_barrier()
#define SCHED() __builtin_amdgcn_sched_barrier(0)

constexpr int X_OFF=0, G_OFF=16384, U_OFF=32768+32, STG=49408, NSTG=3;
constexpr int LDS1=STG*NSTG;

DI unsigned pk_bf16(float a,float b){ f32x2 v={a,b}; bf16x2 r=__builtin_convertvector(v,bf16x2); return __builtin_bit_cast(unsigned,r);}
DI bf16x8 trpair(LAS char* p,int off0,int off1){
  s16x4 a=__builtin_amdgcn_ds_read_tr16_b64_v4i16((LAS s16x4*)(p+off0));
  s16x4 b=__builtin_amdgcn_ds_read_tr16_b64_v4i16((LAS s16x4*)(p+off1));
  return __builtin_shufflevector(a,b,0,1,2,3,4,5,6,7);
}
DI void dma(__amdgpu_buffer_rsrc_t rs,LAS char* dst,int voff,int soff){
  __builtin_amdgcn_raw_ptr_buffer_load_lds(rs,(LAS void*)dst,16,voff,soff,0,0);
}

__global__ __launch_bounds__(512,2) void k_gateup(const void* x,const void* wg,const void* wu,void* ws){
  __shared__ __attribute__((aligned(256))) char smem_[LDS1];
  LAS char* smem=(LAS char*)smem_;
  const int b=blockIdx.x, xcd=b&7, idx=b>>3;
  STAMP(0);
  PADBEG();
  if(idx>=28) return;
  const int mb=(xcd&3)*4+(idx&3), fb=(xcd>>2)*7+(idx>>2);
  const int lane=threadIdx.x&63, w=__builtin_amdgcn_readfirstlane(threadIdx.x>>6);
  if(w>=4){
    const int lw=w-4;
    auto rx=__builtin_amdgcn_make_buffer_rsrc((void*)x,0,S_*D_*2,RSRC3);
    auto rg=__builtin_amdgcn_make_buffer_rsrc((void*)wg,0,D_*F_*2,RSRC3);
    auto ru=__builtin_amdgcn_make_buffer_rsrc((void*)wu,0,D_*F_*2,RSRC3);
    const int xr=(mb*128+32*lw+(lane>>3))*1024;
    const int vx0=xr+(((lane&7)^((lane>>4)))<<4);
    const int vx1=xr+8*1024+(((lane&7)^((lane>>4)+4))<<4);
    const int vw=(16*lw+(lane>>4))*(F_*2)+fb*256+(((lane&15)^((lane>>4)<<2))<<4);
    auto issue=[&](int kt,int slot){
      LAS char* sb=smem+slot*STG;
      dma(rx,sb+X_OFF+lw*4096+0,   vx0,kt*128);
      dma(rx,sb+X_OFF+lw*4096+1024,vx1,kt*128);
      dma(rx,sb+X_OFF+lw*4096+2048,vx0,kt*128+16*1024);
      dma(rx,sb+X_OFF+lw*4096+3072,vx1,kt*128+16*1024);
      #pragma unroll
      for(int i=0;i<4;i++){
        dma(rg,sb+G_OFF+lw*4096+i*1024,vw,(kt*64+4*i)*(F_*2));
        dma(ru,sb+U_OFF+lw*4096+i*1024,vw,(kt*64+4*i)*(F_*2));
      }
    };
    issue(0,0); issue(1,1); issue(2,2);
    WAITV(24); BAR();
    WAITV(12); BAR();
    int slot=0;
    for(int t=2;t<8;t++){
      if(t+1<8){ issue(t+1,slot); WAITV(12);} else { WAITV(0);}
      slot=slot==2?0:slot+1;
      BAR();
    }
    return;
  }
  const int wr=w>>1, wc=w&1;
  const int h=lane>>5, isU=(lane>>4)&1, q=(lane&15)>>2, p=lane&3;
  int aw[2], ax[4];
  #pragma unroll
  for(int ui=0;ui<2;ui++) aw[ui]=(isU?U_OFF:G_OFF)+256*(8*h+q)+64*((2*wc+ui)^q)+8*p;
  { const int row=64*wr+(lane&31);
    #pragma unroll
    for(int s=0;s<4;s++) ax[s]=X_OFF+128*row+16*((2*s+h)^((row>>1)&7)); }
  f32x16 acc[4][2];
  #pragma unroll
  for(int c=0;c<4;c++) for(int m=0;m<2;m++) for(int i=0;i<16;i++) acc[c][m][i]=0.f;
  bf16x8 fa[2][4], fbx[2][2];
  auto rd=[&](int buf,LAS char* sb,int s){
    #pragma unroll
    for(int m=0;m<2;m++) fbx[buf][m]=*(LAS bf16x8*)(sb+ax[s]+m*4096);
    #pragma unroll
    for(int c=0;c<4;c++) fa[buf][c]=trpair(sb+aw[c>>1],256*(16*s)+32*(c&1),256*(16*s+4)+32*(c&1));
  };
  auto mm=[&](int buf){
    #pragma unroll
    for(int c=0;c<4;c++)
      #pragma unroll
      for(int m=0;m<2;m++) acc[c][m]=__builtin_amdgcn_mfma_f32_32x32x16_bf16(fa[buf][c],fbx[buf][m],acc[c][m],0,0,0);
  };
  auto shape=[&](){
    #pragma unroll
    for(int i=0;i<5;i++){ __builtin_amdgcn_sched_group_barrier(0x8,1,0); __builtin_amdgcn_sched_group_barrier(0x100,2,0);}
    __builtin_amdgcn_sched_group_barrier(0x8,3,0);
  };
  BAR();
  STAMP(1);
  int slot=0;
  rd(0,smem,0);
  for(int t=0;t<8;t++){
    LAS char* sb=smem+slot*STG;
    SCHED();
    rd(1,sb,1); mm(0); shape(); SCHED();
    rd(0,sb,2); mm(1); shape(); SCHED();
    rd(1,sb,3); mm(0); shape(); SCHED();
    slot=slot==2?0:slot+1;
    if(t<7){
      WAITL(0); BAR(); SCHED();
      rd(0,smem+slot*STG,0); mm(1); shape(); SCHED();
    } else mm(1);
  }
  STAMP(2);
  auto rh=__builtin_amdgcn_make_buffer_rsrc(ws,0,S_*F_*2,RSRC3);
  #pragma unroll
  for(int m=0;m<2;m++)
    #pragma unroll
    for(int c=0;c<4;c++){
      u32x4 o;
      #pragma unroll
      for(int e=0;e<4;e++){
        unsigned gp=pk_bf16(acc[c][m][2*e],acc[c][m][2*e+1]);
        unsigned up=pk_bf16(acc[c][m][8+2*e],acc[c][m][8+2*e+1]);
        s16x2 z={0,0};
        gp=__builtin_bit_cast(unsigned,__builtin_elementwise_max(__builtin_bit_cast(s16x2,gp),z));
        float g0=__builtin_bit_cast(float,gp<<16), g1=__builtin_bit_cast(float,gp&0xffff0000u);
        float u0=__builtin_bit_cast(float,up<<16), u1=__builtin_bit_cast(float,up&0xffff0000u);
        o[e]=pk_bf16(g0*u0,g1*u1);
      }
      const int MT=mb*4+wr*2+m, KS=fb*8+wc*4+c;
      __builtin_amdgcn_raw_buffer_store_b128(o,rh,(MT*112+KS)*1024+lane*16,0,0);
    }
  STAMP(3);
  PADEND(PAD1);
}

constexpr int QREG=40960;
__global__ __launch_bounds__(512,2) void k_down(const void* wd,void* ws,void* out){
  __shared__ __attribute__((aligned(256))) char smem_[163840];
  LAS char* smem=(LAS char*)smem_;
  const int b=blockIdx.x, xcd=b&7, idx=b>>3;
  STAMP(4);
  PADBEG();
  const int mi=xcd*4+(idx&3), ni=idx>>2;
  const int lane=threadIdx.x&63, w=__builtin_amdgcn_readfirstlane(threadIdx.x>>6);
  const int kq=w&3, mh=w>>2;
  auto rd_=__builtin_amdgcn_make_buffer_rsrc((void*)wd,0,F_*D_*2,RSRC3);
  auto rh=__builtin_amdgcn_make_buffer_rsrc(ws,0,S_*F_*2,RSRC3);
  LAS char* qb=smem+kq*QREG;
  const int vd=(kq*448+(lane>>3))*1024+ni*128+(((lane&7)^(((lane>>4)&1)<<2))<<4);
  #pragma unroll
  for(int i=0;i<12;i++) dma(rd_,qb+(mh*12+i)*1024,vd,(mh*12+i)*8*1024);
  #pragma unroll
  for(int i=0;i<8;i++) dma(rd_,qb+24576+(mh*8+i)*1024,vd,(24+mh*8+i)*8*1024);
  u32x4 hf[28];
  const int MT=mi*2+mh;
  #pragma unroll
  for(int i=0;i<28;i++) hf[i]=__builtin_amdgcn_raw_buffer_load_b128(rh,lane*16,(MT*112+kq*28+i)*1024,0);
  SCHED();
  const int h=lane>>5, blk=(lane>>4)&1, q=(lane&15)>>2, p=lane&3;
  int ad[2];
  #pragma unroll
  for(int nt=0;nt<2;nt++) ad[nt]=128*(4*h+q)+64*(nt^(q>>1))+32*blk+8*p;
  WAITV(36); BAR();
  bf16x8 wa[12][2];
  #pragma unroll
  for(int ks=0;ks<12;ks++)
    #pragma unroll
    for(int nt=0;nt<2;nt++) wa[ks][nt]=trpair(qb+ad[nt],128*16*ks,128*(16*ks+8));
  WAITL(0); SCHED(); BAR();
  #pragma unroll
  for(int i=0;i<8;i++) dma(rd_,qb+(mh*8+i)*1024,vd,(40+mh*8+i)*8*1024);
  f32x16 acc[2];
  for(int i=0;i<16;i++){acc[0][i]=0.f;acc[1][i]=0.f;}
  #pragma unroll
  for(int ks=0;ks<12;ks++)
    #pragma unroll
    for(int nt=0;nt<2;nt++) acc[nt]=__builtin_amdgcn_mfma_f32_32x32x16_bf16(wa[ks][nt],__builtin_bit_cast(bf16x8,hf[ks]),acc[nt],0,0,0);
  WAITV(0); BAR();
  #pragma unroll
  for(int ks=12;ks<28;ks++){
    const int base=ks<20?24576+(ks-12)*2048:(ks-20)*2048;
    #pragma unroll
    for(int nt=0;nt<2;nt++){ bf16x8 a=trpair(qb+ad[nt],base,base+1024);
      acc[nt]=__builtin_amdgcn_mfma_f32_32x32x16_bf16(a,__builtin_bit_cast(bf16x8,hf[ks]),acc[nt],0,0,0);}
  }
  WAITL(0); BAR();
  STAMP(5);
  { LAS char* pw=smem+kq*(64*272)+(mh*32+(lane&31))*272+16*h;
    #pragma unroll
    for(int nt=0;nt<2;nt++)
      #pragma unroll
      for(int g=0;g<4;g++){ f32x4 v={acc[nt][4*g],acc[nt][4*g+1],acc[nt][4*g+2],acc[nt][4*g+3]}; *(LAS f32x4*)(pw+nt*128+g*32)=v; }
  }
  WAITL(0); BAR();
  { const int row=8*w+(lane>>3); LAS char* pr=smem+row*272+(lane&7)*32;
    f32x4 a={0,0,0,0},c={0,0,0,0};
    #pragma unroll
    for(int k=0;k<4;k++){ a+=*(LAS f32x4*)(pr+k*64*272); c+=*(LAS f32x4*)(pr+k*64*272+16);}
    u32x4 o={pk_bf16(a[0],a[1]),pk_bf16(a[2],a[3]),pk_bf16(c[0],c[1]),pk_bf16(c[2],c[3])};
    *(u32x4*)((char*)out+(size_t)(mi*64+row)*1024+ni*128+(lane&7)*16)=o;
  }
  STAMP(6);
  PADEND(PAD2);
}

extern "C" void kernel_launch(void* const* d_in,const int* in_sizes,int n_in,void* d_out,int out_size,void* d_ws,size_t ws_size,hipStream_t stream){
  k_gateup<<<256,512,0,stream>>>(d_in[0],d_in[1],d_in[2],d_ws);
  k_down<<<256,512,0,stream>>>(d_in[3],d_ws,d_out);
}
```

```cpp
#include <hip/hip_runtime.h>
#include <hip/hip_bf16.h>
#include <type_traits>
#define LAS __attribute__((address_space(3)))
#define GAS __attribute__((address_space(1)))
typedef short s16x4 __attribute__((ext_vector_type(4)));
typedef short bf16x8 __attribute__((ext_vector_type(8)));
typedef float f32x2 __attribute__((ext_vector_type(2)));
typedef float f32x4 __attribute__((ext_vector_type(4)));
typedef float f32x16 __attribute__((ext_vector_type(16)));
typedef __bf16 bf16x2 __attribute__((ext_vector_type(2)));
typedef unsigned u32x4 __attribute__((ext_vector_type(4)));
typedef unsigned long long u64;
typedef GAS u64 gu64;
#define DI __device__ __forceinline__
#define RLX_AGENT __ATOMIC_RELAXED, __HIP_MEMORY_SCOPE_AGENT
#define WAITV(n) asm volatile("s_waitcnt vmcnt(" #n ")" ::: "memory")
#define WAITL(n) asm volatile("s_waitcnt lgkmcnt(" #n ")" ::: "memory")
#define BAR() do { asm volatile("" ::: "memory"); __builtin_amdgcn_s_barrier(); asm volatile("" ::: "memory"); } while (0)
#define SCHED() __builtin_amdgcn_sched_barrier(0)
#define SGB(m, n) __builtin_amdgcn_sched_group_barrier((m), (n), 0)

constexpr int S_ = 2048, D_ = 512, F_ = 1792;
constexpr int RSRC3 = 0x00020000, AUX_SC1 = 16;
constexpr size_t CTRL_OFF = 8u << 20;
constexpr int SL = 16384;
constexpr int RN = 4;
constexpr int HW = 10;
constexpr int TB = 8;
constexpr int CNR = 16, CN1 = 8;
constexpr int XJ = 4;
static_assert(XJ >= 4 && XJ <= 22 && CN1 >= 4, "barrier X placement");
constexpr int XSL[8] = {0, 3, 6, 9, 2, 4, 6, 8}, ASL[8] = {1, 4, 7, 0, 3, 5, 7, 9}, BSL[8] = {2, 5, 8, 1, 0, 7, 9, 1};
constexpr int WS[14] = {1, 3, 5, 3, 5, 2, 0, 4, 7, 6, 9, 8, 1, 3};
constexpr int xs_(int st) { return XSL[st < 8 ? st : st - 4]; }
constexpr int as_(int st) { return st < 8 ? ASL[st] : -1; }
constexpr int bs_(int st) { return st < 4 ? BSL[st] : (st < 8 ? -1 : BSL[st - 4]); }

DI unsigned pk_bf16(float a, float b) { f32x2 v = {a, b}; return __builtin_bit_cast(unsigned, __builtin_convertvector(v, bf16x2)); }
DI bf16x8 trpair(LAS char* p, int off0, int off1) {
  s16x4 a = __builtin_amdgcn_ds_read_tr16_b64_v4i16((LAS s16x4*)(p + off0));
  s16x4 b = __builtin_amdgcn_ds_read_tr16_b64_v4i16((LAS s16x4*)(p + off1));
  return __builtin_shufflevector(a, b, 0, 1, 2, 3, 4, 5, 6, 7);
}
DI u32x4 mkrsrc(const void* p, unsigned bytes) { u64 a = (u64)p; return u32x4{(unsigned)a, (unsigned)(a >> 32) & 0xffffu, bytes, (unsigned)RSRC3}; }
DI void dma4(u32x4 rs, int lds, const int (&v)[4], int soff) {
  asm volatile("s_mov_b32 m0, %0\n\ts_nop 0\n\t"
               "buffer_load_dwordx4 %1, %5, %6 offen lds\n\t"
               "buffer_load_dwordx4 %2, %5, %6 offen offset:1024 lds\n\t"
               "buffer_load_dwordx4 %3, %5, %6 offen offset:2048 lds\n\t"
               "buffer_load_dwordx4 %4, %5, %6 offen offset:3072 lds"
               :: "s"(lds), "v"(v[0]), "v"(v[1]), "v"(v[2]), "v"(v[3]), "s"(rs), "s"(soff) : "memory");
}
DI f32x4 mfma(bf16x8 a, bf16x8 b, f32x4 c) { return __builtin_amdgcn_mfma_f32_16x16x32_bf16(a, b, c, 0, 0, 0); }
DI f32x16 mfma32(bf16x8 a, bf16x8 b, f32x16 c) { return __builtin_amdgcn_mfma_f32_32x32x16_bf16(a, b, c, 0, 0, 0); }
DI float rbf(float a) { return __builtin_bit_cast(float, pk_bf16(0.f, a)); }
DI float relu(float a) { int v = __builtin_bit_cast(int, a); return __builtin_bit_cast(float, v > 0 ? v : 0); }
DI unsigned glu2(float g0, float g1, float u0, float u1, float nz) {
  return pk_bf16(rbf(relu(g0)) * rbf(u0), __builtin_fmaf(rbf(relu(g1)), rbf(u1), nz));
}
DI u64 next_tag(u64 v) {
  const u64 g = ((u64)(unsigned)__builtin_amdgcn_readfirstlane((unsigned)(v >> 32)) << 32) | (unsigned)__builtin_amdgcn_readfirstlane((unsigned)v);
  return g * 0x9E3779B97F4A7C15ull + 0xD1B54A32D192ED03ull;
}

__global__ __launch_bounds__(512, 2) void reglu_fused(const void* x, const void* wg, const void* wu, const void* wd, void* ws, void* out) {
  __shared__ __attribute__((aligned(256))) char smem_[10 * SL];
  LAS char* smem = (LAS char*)smem_;
  const int lbase = (int)(size_t)smem;
  SCHED();
  const int b = blockIdx.x, xcd = b & 7, idx = b >> 3;
  const int lane = threadIdx.x & 63;
  int w = __builtin_amdgcn_readfirstlane(threadIdx.x >> 6);
  u64 pc0; asm volatile("s_getpc_b64 %0\nreglu_code_begin:" : "=s"(pc0));
  const int mb = (xcd & 3) * 4 + (idx & 3), jh = xcd >> 2;
  int jl = idx >> 2;

  int vx[4], vw[4];
  auto setupx = [&](int l) {
    #pragma unroll
    for (int i = 0; i < 4; i++) {
      vx[i] = (mb * 128 + 32 * (w & 3) + 8 * i + (l >> 3)) * 1024 - 1024 * i + (((l & 7) ^ ((l >> 4) + 4 * (i & 1))) << 4);
      vw[i] = (32 * (w & 1) + 8 * i + (l >> 3)) * (F_ * 2) - 1024 * i + (jh * 7 + jl) * 256 + (((l & 7) ^ ((((l >> 4) & 1) | ((i & 1) << 1)) << 1)) << 4);
      asm volatile("" : "+v"(vx[i]), "+v"(vw[i]));
    }
  };
  setupx(lane);
  const int kx = ((idx + jl) & 1) ^ (((xcd ^ jh) & 1) << 2);
  const int lb = lbase + (w & 3) * 4096;
  SCHED();
  const u32x4 rx = mkrsrc(x, S_ * D_ * 2), rw = mkrsrc((w & 2) ? wu : wg, D_ * F_ * 2);
  auto LX = [&](int t) { dma4(rx, lb + XSL[t] * SL, vx, (t ^ kx) * 128); };
  auto LA = [&](int t) { dma4(rw, lb + ASL[t] * SL, vw, (t ^ kx) * 64 * (F_ * 2)); };
  auto LB = [&](int t) { dma4(rw, lb + BSL[t] * SL, vw, (t ^ kx) * 64 * (F_ * 2) + 128); };
  if (__builtin_expect(w >= 4 && jl < 7, 1)) { LX(0); LA(0); LB(0); LX(1); LA(1); }
  asm volatile("" : "+s"(w), "+s"(jl));

  const bool prod = jl < 7;
  const int fb = jh * 7 + jl, me = jh * 8 + jl;
  const int ni = (jl + 4 - (int)((size_t)wd >> 7)) & 7;
  auto rh = __builtin_amdgcn_make_buffer_rsrc(ws, 0, S_ * F_ * 2, RSRC3);
  gu64* ctrl = (gu64*)((GAS char*)ws + CTRL_OFF + mb * 8192);
  gu64* const myfl = ctrl + (me >> 2) * 16;
  const int kh = (w & 3) >> 1, mh = w & 1;
  int rot = xcd == 7 ? 0 : xcd;
  auto pfb = [&](int kh_, int i) { int t = i + rot; return kh_ * 7 + (t >= 7 ? t - 7 : t); };
  LAS unsigned* const cnt = (LAS unsigned*)(smem + WS[13] * SL);

  int ln, ad[2], hso[7];
  f32x16 acc[2];
  auto setup2 = [&]() {
    ln = __builtin_amdgcn_mbcnt_hi(~0u, __builtin_amdgcn_mbcnt_lo(~0u, 0u)); asm volatile("" : "+v"(ln));
    #pragma unroll
    for (int nt = 0; nt < 2; nt++) ad[nt] = kh * 8192 + 128 * (8 * (ln >> 5) + ((ln & 15) >> 2)) + 64 * (nt ^ ((ln >> 3) & 1)) + 32 * ((ln >> 4) & 1) + 8 * (ln & 3);
  };
  auto setuph = [&](int half) {
    #pragma unroll
    for (int i = 0; i < 7; i++) { hso[i] = ((mb * 4 + jh * 2 + mh) * 112 + pfb(kh, i) * 8 + half * 4) * 1024; asm volatile("" : "+s"(hso[i])); }
  };
  auto rdws = [&](int slot, int un, int nt) { return trpair(smem + slot * SL + ad[nt], 2048 * un, 2048 * un + 512); };
  auto poll = [&](int set, u64 tag) {
    unsigned spins = 0;
    for (;;) {
      u64 v = tag;
      if (ln < 8) { u64 z = 0; asm volatile("global_atomic_or_x2 %0, %1, %2, off sc0 sc1\n\ts_waitcnt vmcnt(0)" : "=&v"(v) : "v"(myfl + set * 256 + kh * 8 + ln), "v"(z) : "memory"); }
      if (__all(v == tag)) break;
      if (++spins > (1u << 20)) break;
      __builtin_amdgcn_s_sleep(4);
    }
  };
  auto gemm2 = [&](auto L_, auto& wa, auto&& mid) {
    constexpr bool LD = decltype(L_)::value;
    u32x4 hf[28]; bf16x8 wb[28][2];
    auto ldh = [&](int j) { hf[j] = __builtin_amdgcn_raw_buffer_load_b128(rh, ln * 16 + (j & 3) * 1024, hso[j >> 2], AUX_SC1); };
    auto rdw = [&](int j) { if (j >= 16) { wb[j][0] = rdws(WS[(LD ? 0 : 7) + (j >> 2)], j & 3, 0); wb[j][1] = rdws(WS[(LD ? 0 : 7) + (j >> 2)], j & 3, 1); } };
    #pragma unroll
    for (int j = 0; j < HW; j++) ldh(j);
    SCHED();
    mid();
    #pragma unroll
    for (int i = 0; i < 16; i++) { acc[0][i] = 0.f; acc[1][i] = 0.f; }
    SCHED();
    #pragma unroll
    for (int j = 0; j < 28; j++) {
      if (!LD && j == XJ) { BAR(); SCHED(); }
      if (j + 2 < 28) rdw(j + 2);
      #pragma unroll
      for (int nt = 1; nt >= 0; nt--) acc[nt] = mfma32(j < 16 ? wa[j < 16 ? j : 0][nt] : wb[j][nt], __builtin_bit_cast(bf16x8, hf[j]), acc[nt]);
      if (j + HW < 28) ldh(j + HW);
      SGB(0x8, 1); SGB(0x20, 1); SGB(0x100, 1); SGB(0x8, 1); SGB(0x100, 3);
      SCHED();
    }
  };
  auto putp = [&](int slot) {
    LAS char* pw = smem + slot * SL + (mh * 32 + (ln & 31)) * 256;
    #pragma unroll
    for (int nt = 0; nt < 2; nt++)
      #pragma unroll
      for (int i = 0; i < 4; i++)
        *(LAS f32x4*)(pw + 16 * ((8 * nt + 2 * i + (ln >> 5)) ^ (ln & 7))) = f32x4{acc[nt][4 * i], acc[nt][4 * i + 1], acc[nt][4 * i + 2], acc[nt][4 * i + 3]};
  };
  auto getp = [&](int slot, int c) { return *(LAS f32x4*)(smem + slot * SL + (8 * w + (ln >> 3)) * 256 + 16 * ((2 * (ln & 7) + c) ^ (ln >> 3))); };
  auto finish = [&]() {
    const f32x4 a = (getp(WS[4], 0) + getp(WS[5], 0)) + (getp(WS[6], 0) + getp(WS[7], 0));
    const f32x4 c = (getp(WS[4], 1) + getp(WS[5], 1)) + (getp(WS[6], 1) + getp(WS[7], 1));
    const u32x4 o = {pk_bf16(a[0], a[1]), pk_bf16(a[2], a[3]), pk_bf16(c[0], c[1]), pk_bf16(c[2], c[3])};
    auto ro = __builtin_amdgcn_make_buffer_rsrc(out, 0, S_ * D_ * 2, RSRC3);
    __builtin_amdgcn_raw_buffer_store_b128(o, ro, (mb * 128 + jh * 64 + 8 * w + (ln >> 3)) * 1024 + ni * 128 + (ln & 7) * 16, 0, AUX_SC1);
  };

  if (w >= 4) {
    setup2(); setupx(ln);
    const u32x4 rwd = mkrsrc(wd, F_ * D_ * 2);
    int vd[4], lbw = lb;
    #pragma unroll
    for (int i = 0; i < 4; i++) vd[i] = (32 * mh + 8 * i + (ln >> 3)) * 1024 - 1024 * i + ni * 128 + (((ln & 7) ^ (((ln >> 4) & 1) << 2)) << 4);
    auto LWs = [&](int i, int slot) { dma4(rwd, lbw + slot * SL, vd, (i < 7 ? pfb(kh, i) * 128 : pfb(kh, i - 7) * 128 + 64) * 1024); };
    auto LW = [&](int i) { LWs(i, WS[i]); };
    bf16x8 wa[16][2];
    auto rdR = [&](int k, int slot) {
      #pragma unroll
      for (int j = 4 * k; j < 4 * k + 4; j++) { wa[j][0] = rdws(slot, j & 3, 0); wa[j][1] = rdws(slot, j & 3, 1); }
    };
    u64 tag;
    __builtin_amdgcn_s_setprio(3);
    if (prod) {
      u64 gen;
      WAITV(8); BAR();
      asm volatile("global_load_dwordx2 %0, %1, off sc1" : "=v"(gen) : "v"(ctrl + 512) : "memory");
      LB(1); LX(2); LA(2); LB(2);
      asm volatile("s_waitcnt vmcnt(12)" : "+v"(gen) :: "memory"); BAR();
      tag = next_tag(gen);
      LX(3); LA(3); LB(3); LX(4); WAITV(16); BAR();
      LA(4); LX(5); LA(5); WAITV(16); BAR();
      LX(6); LA(6); LX(7); WAITV(20); BAR();
      LA(7); LB(4); LW(0); WAITV(24); BAR();
      LW(1); WAITV(20); BAR();
      LW(2); WAITV(16); BAR();
      LB(5); WAITV(4); BAR();
      LB(6); rdR(0, WS[0]); rdR(1, WS[1]); rdR(2, WS[2]); WAITL(0); SCHED(); BAR();
      LB(7); LW(3); LW(4); WAITV(16); BAR();
      LW(5); LW(6); WAITV(12); BAR();
      LW(7); LW(8); rdR(3, WS[3]);
      if (w == 4 && ln < RN) __hip_atomic_store(ctrl + ln * 16 + me, tag, RLX_AGENT);
      WAITL(0); SCHED(); BAR();
      LW(9); LW(10);
      if (w == 5) *cnt = 0u;
      WAITL(0); SCHED(); BAR();
      LW(11);
    } else {
      tag = next_tag(__hip_atomic_load(ctrl + 512, RLX_AGENT));
      asm volatile("" : "+s"(tag));
      BAR();
      LWs(0, WS[9]); LWs(1, WS[10]); LWs(2, WS[11]); LWs(3, WS[12]); LW(4); LW(5); LW(6); LW(7); LW(8);
      WAITV(20); BAR();
      rdR(0, WS[9]); rdR(1, WS[10]); rdR(2, WS[11]); rdR(3, WS[12]);
      WAITL(0); SCHED(); BAR();
      LW(9); LW(10); LW(11);
    }
    asm volatile("" : "+s"(lbw), "+s"(rot));
    __builtin_amdgcn_s_setprio(0);
    setuph(0);
    u64 pa = tag;
    if (ln < 8) { u64 z = 0; pa = ~tag; asm volatile("global_atomic_or_x2 %0, %1, %2, off sc0 sc1\n\ts_nop 1" : "+v"(pa) : "v"(myfl + kh * 8 + ln), "v"(z) : "memory"); }
    LW(12);
    asm volatile("s_waitcnt vmcnt(4)" : "+v"(pa) :: "memory");
    if (!__all(pa == tag)) poll(0, tag);
    __builtin_amdgcn_fence(__ATOMIC_ACQUIRE, "wavefront");
    gemm2(std::true_type{}, wa, [&]() {
      asm volatile("s_waitcnt vmcnt(%0)" :: "n"(HW) : "memory");
      BAR();
      LW(13); });
    WAITV(0); WAITL(0); BAR();
    putp(kh ? WS[5] : WS[4]);
    if (w == 4 && me == 0 && ln == 0) __hip_atomic_store(ctrl + 512, tag, RLX_AGENT);
    WAITL(0); BAR();
    finish();
  } else {
    u64 tag;
    if (prod) {
      u64 gen;
      asm volatile("global_load_dwordx2 %0, %1, off sc1" : "=v"(gen) : "v"(ctrl + 512) : "memory");
      const int g = lane >> 4, q = (lane & 15) >> 2, p = lane & 3, row = lane & 15;
      int awb[3], axb[2][3];
      #pragma unroll
      for (int k = 0; k < 3; k++) {
        awb[k] = lbase + k * 65536 + 128 * (8 * g + q) + 32 * (w ^ ((q >> 1) | ((g & 1) << 1))) + 8 * p;
        axb[0][k] = lbase + k * 65536 + 128 * row + 16 * (g ^ ((row >> 1) & 7));
        axb[1][k] = lbase + k * 65536 + 128 * row + 16 * ((4 + g) ^ ((row >> 1) & 7));
        asm volatile("" : "+v"(awb[k]), "+v"(axb[0][k]), "+v"(axb[1][k]));
      }
      f32x4 ag[2][8], au[2][8];
      #pragma unroll
      for (int c = 0; c < 2; c++) for (int m = 0; m < 8; m++) { ag[c][m] = f32x4{0, 0, 0, 0}; au[c][m] = f32x4{0, 0, 0, 0}; }
      bf16x8 fg[2][2], fu[2][2], fx[2][8];
      auto rd = [&](int st, int s) {
        if (as_(st) >= 0) { LAS char* pb = (LAS char*)(size_t)awb[as_(st) >> 2]; const int o = (as_(st) & 3) * SL + 4096 * s; fg[s][0] = trpair(pb, o, o + 512); fu[s][0] = trpair(pb, o + 8192, o + 8192 + 512); }
        if (bs_(st) >= 0) { LAS char* pb = (LAS char*)(size_t)awb[bs_(st) >> 2]; const int o = (bs_(st) & 3) * SL + 4096 * s; fg[s][1] = trpair(pb, o, o + 512); fu[s][1] = trpair(pb, o + 8192, o + 8192 + 512); }
        #pragma unroll
        for (int m = 0; m < 8; m++) fx[s][m] = *(LAS bf16x8*)((LAS char*)(size_t)axb[s][xs_(st) >> 2] + (xs_(st) & 3) * SL + m * 2048);
      };
      auto mm1 = [&](int s, int c, int m) { ag[c][m] = mfma(fg[s][c], fx[s][m], ag[c][m]); au[c][m] = mfma(fu[s][c], fx[s][m], au[c][m]); };
      auto mm = [&](int st, int s) {
        #pragma unroll
        for (int m = 0; m < 8; m++) { if (as_(st) >= 0) mm1(s, 0, m); if (bs_(st) >= 0) mm1(s, 1, m); }
      };
      auto shape = [&](int nm, int nd) {
        if (nm == 32) {
          #pragma unroll
          for (int i = 0; i < nd - 8; i++) { SGB(0x8, 1); SGB(0x100, 1); }
          #pragma unroll
          for (int i = 0; i < 8; i++) { SGB(0x8, 2); SGB(0x100, 1); }
          SGB(0x8, 16);
        } else {
          #pragma unroll
          for (int i = 0; i < 16; i++) { SGB(0x8, 1); if (i < nd) SGB(0x100, 1); }
        }
      };
      int vst = (g >> 1) * 512 + (g & 1) * 256 + row * 16 + (mb * 448 + fb * 8 + w) * 1024;
      asm volatile("" : "+v"(vst));
      float nz = -0.0f; asm volatile("" : "+s"(nz));
      auto epi = [&](int c, int mp) {
        const int m = 2 * mp;
        unsigned a0 = glu2(ag[c][m][0], ag[c][m][1], au[c][m][0], au[c][m][1], nz);
        unsigned a1 = glu2(ag[c][m][2], ag[c][m][3], au[c][m][2], au[c][m][3], nz);
        unsigned c0 = glu2(ag[c][m + 1][0], ag[c][m + 1][1], au[c][m + 1][0], au[c][m + 1][1], nz);
        unsigned c1 = glu2(ag[c][m + 1][2], ag[c][m + 1][3], au[c][m + 1][2], au[c][m + 1][3], nz);
        auto r0 = __builtin_amdgcn_permlane16_swap(a0, c0, false, false);
        auto r1 = __builtin_amdgcn_permlane16_swap(a1, c1, false, false);
        __builtin_amdgcn_raw_buffer_store_b128(u32x4{r0[0], r1[0], r0[1], r1[1]}, rh, vst + (mp * 112 + c * 4) * 1024, 0, AUX_SC1);
      };
      #pragma unroll
      for (int m = 0; m < 8; m++) asm volatile("" : "+v"(ag[0][m]), "+v"(ag[1][m]), "+v"(au[0][m]), "+v"(au[1][m]));
      BAR();
      rd(0, 0);
      #pragma unroll
      for (int st = 0; st < 11; st++) {
        const int nm = st < 4 ? 32 : 16;
        SCHED();
        rd(st, 1); mm(st, 0); shape(nm, st < 4 ? 16 : 12);
        SCHED();
        if (st == 1) { asm volatile("s_waitcnt vmcnt(0)" : "+v"(gen) :: "memory"); tag = next_tag(gen); }
        if (st == 9) WAITV(0);
        WAITL(0); BAR();
        SCHED();
        rd(st + 1, 0);
        if (st == 7) {
          #pragma unroll
          for (int m = 0; m < 4; m++) mm1(1, 0, m);
          #pragma unroll
          for (int i = 0; i < 8; i++) { SGB(0x8, 1); SGB(0x100, 1); if (i < 4) SGB(0x100, 1); }
          SCHED();
          #pragma unroll
          for (int m = 4; m < 8; m++) mm1(1, 0, m);
          epi(0, 0); epi(0, 1);
          #pragma unroll
          for (int i = 0; i < 8; i++) { SGB(0x8, 1); SGB(0x2, 9); }
          SCHED();
          epi(0, 2); epi(0, 3);
          SCHED();
          BAR();
        } else { mm(st, 1); shape(nm, st + 1 < 4 ? 16 : 12); }
        SCHED();
      }
      rd(11, 1);
      #pragma unroll
      for (int m = 0; m < 6; m++) mm1(0, 1, m);
      #pragma unroll
      for (int i = 0; i < 12; i++) { SGB(0x8, 1); SGB(0x100, 1); }
      SCHED();
      mm1(1, 1, 0); mm1(1, 1, 1); SCHED();
      WAITL(0); BAR(); SCHED();
      #pragma unroll
      for (int mp = 0; mp < 4; mp++) {
        if (mp < 3) { if (mp < 2) mm1(0, 1, 6 + mp); mm1(1, 1, 2 * mp + 2); mm1(1, 1, 2 * mp + 3); }
        epi(1, mp);
        if (mp < 2) {
          #pragma unroll
          for (int i = 0; i < 6; i++) { SGB(0x8, 1); SGB(0x2, 6); }
        } else if (mp < 3) {
          #pragma unroll
          for (int i = 0; i < 4; i++) { SGB(0x8, 1); SGB(0x2, 9); }
        }
        SCHED();
      }
    } else {
      const u64 gen = __hip_atomic_load(ctrl + 512, RLX_AGENT);
      unsigned keep[TB + 1];
      { unsigned csz; asm volatile("s_mov_b32 %0, reglu_code_end-reglu_code_begin" : "=s"(csz));
        auto rc = __builtin_amdgcn_make_buffer_rsrc((void*)pc0, 0, csz, RSRC3);
        keep[TB] = __builtin_amdgcn_raw_buffer_load_b32(rc, lane * 128 + w * 8192, 0, 0); }
      auto rr = __builtin_amdgcn_make_buffer_rsrc((void*)wd, 0, F_ * D_ * 2, RSRC3);
      const int n = ((idx & 3) * 4 + w) * 64 + lane;
      #pragma unroll
      for (int i = 0; i < TB; i++) keep[i] = __builtin_amdgcn_raw_buffer_load_b32(rr, (n & 511) * 128 + (n >> 9) * (7 * 131072), (pfb(0, i < 7 ? i : i - 7) * 128 + (i < 7 ? 0 : 64)) * 1024, 0);
      SCHED();
      tag = next_tag(gen);
      asm volatile("" : "+s"(tag));
      BAR();
      if (threadIdx.x < 32 && (threadIdx.x & 15) < RN) __hip_atomic_store(ctrl + threadIdx.x * 16 + me, tag, RLX_AGENT);
      BAR(); BAR();
      #pragma unroll
      for (int i = 0; i <= TB; i++) asm volatile("" :: "v"(keep[i]));
    }
    setup2(); setuph(1);
    WAITV(0);
    if (prod) {
      unsigned old = 0;
      if (ln == 0) old = __hip_atomic_fetch_add(cnt, 1u, __ATOMIC_RELAXED, __HIP_MEMORY_SCOPE_WORKGROUP);
      if (__builtin_amdgcn_readfirstlane(old) == 3 && ln < RN) __hip_atomic_store(ctrl + 256 + ln * 16 + me, tag, RLX_AGENT);
    }
    BAR();
    bf16x8 wa[CNR][2];
    auto rdC = [&](int j0, int j1) {
      #pragma unroll
      for (int j = j0; j < j1; j++) { wa[j][0] = rdws(WS[7 + (j >> 2)], j & 3, 0); wa[j][1] = rdws(WS[7 + (j >> 2)], j & 3, 1); }
    };
    rdC(0, CN1);
    SCHED();
    __builtin_amdgcn_s_sleep(4);
    poll(1, tag);
    __builtin_amdgcn_fence(__ATOMIC_ACQUIRE, "wavefront");
    gemm2(std::false_type{}, wa, [&]() { rdC(CN1, CNR); });
    putp(kh ? WS[7] : WS[6]);
    WAITL(0); BAR();
    finish();
  }
  asm volatile("reglu_code_end:");
}

extern "C" void kernel_launch(void* const* d_in, const int* in_sizes, int n_in, void* d_out, int out_size,
                              void* d_ws, size_t ws_size, hipStream_t stream) {
  reglu_fused<<<256, 512, 0, stream>>>(d_in[0], d_in[1], d_in[2], d_in[3], d_ws, d_out);
}
```

```cpp
#include <hip/hip_runtime.h>
#include <hip/hip_bf16.h>
#include <type_traits>
#define LAS __attribute__((address_space(3)))
#define GAS __attribute__((address_space(1)))
typedef short s16x4 __attribute__((ext_vector_type(4)));
typedef short bf16x8 __attribute__((ext_vector_type(8)));
typedef float f32x2 __attribute__((ext_vector_type(2)));
typedef float f32x4 __attribute__((ext_vector_type(4)));
typedef float f32x16 __attribute__((ext_vector_type(16)));
typedef __bf16 bf16x2 __attribute__((ext_vector_type(2)));
typedef unsigned u32x4 __attribute__((ext_vector_type(4)));
typedef unsigned long long u64;
typedef GAS u64 gu64;
#define DI __device__ __forceinline__
#define RLX_AGENT __ATOMIC_RELAXED, __HIP_MEMORY_SCOPE_AGENT
#define WAITV(n) asm volatile("s_waitcnt vmcnt(" #n ")" ::: "memory")
#define WAITL(n) asm volatile("s_waitcnt lgkmcnt(" #n ")" ::: "memory")
#define BAR() do { asm volatile("" ::: "memory"); __builtin_amdgcn_s_barrier(); asm volatile("" ::: "memory"); } while (0)
#define SCHED() __builtin_amdgcn_sched_barrier(0)
#define SGB(m, n) __builtin_amdgcn_sched_group_barrier((m), (n), 0)

constexpr int S_ = 2048, D_ = 512, F_ = 1792;
constexpr int RSRC3 = 0x00020000, AUX_SC1 = 16;
constexpr size_t CTRL_OFF = 8u << 20;
constexpr int SL = 16384;
constexpr int RN = 4;
constexpr int HW = 10;
constexpr int TB = 8;
constexpr int CNR = 16, CN1 = 8;
constexpr int XJ = 4;
static_assert(XJ >= 4 && XJ <= 22 && CN1 >= 4, "barrier X placement");
constexpr int XSL[8] = {0, 3, 6, 9, 2, 4, 6, 8}, ASL[8] = {1, 4, 7, 0, 3, 5, 7, 9}, BSL[8] = {2, 5, 8, 1, 0, 7, 9, 1};
constexpr int WS[14] = {1, 3, 5, 3, 5, 2, 0, 4, 7, 6, 9, 8, 1, 3};
constexpr int xs_(int st) { return XSL[st < 8 ? st : st - 4]; }
constexpr int as_(int st) { return st < 8 ? ASL[st] : -1; }
constexpr int bs_(int st) { return st < 4 ? BSL[st] : (st < 8 ? -1 : BSL[st - 4]); }

DI unsigned pk_bf16(float a, float b) { f32x2 v = {a, b}; return __builtin_bit_cast(unsigned, __builtin_convertvector(v, bf16x2)); }
DI bf16x8 trpair(LAS char* p, int off0, int off1) {
  s16x4 a = __builtin_amdgcn_ds_read_tr16_b64_v4i16((LAS s16x4*)(p + off0));
  s16x4 b = __builtin_amdgcn_ds_read_tr16_b64_v4i16((LAS s16x4*)(p + off1));
  return __builtin_shufflevector(a, b, 0, 1, 2, 3, 4, 5, 6, 7);
}
DI u32x4 mkrsrc(const void* p, unsigned bytes) { u64 a = (u64)p; return u32x4{(unsigned)a, (unsigned)(a >> 32), bytes, (unsigned)RSRC3}; }
DI void dma4(u32x4 rs, int lds, const int (&v)[4], int soff) {
  asm volatile("s_mov_b32 m0, %0\n\ts_nop 0\n\t"
               "buffer_load_dwordx4 %1, %5, %6 offen lds\n\t"
               "buffer_load_dwordx4 %2, %5, %6 offen offset:1024 lds\n\t"
               "buffer_load_dwordx4 %3, %5, %6 offen offset:2048 lds\n\t"
               "buffer_load_dwordx4 %4, %5, %6 offen offset:3072 lds"
               :: "s"(lds), "v"(v[0]), "v"(v[1]), "v"(v[2]), "v"(v[3]), "s"(rs), "s"(soff) : "memory");
}
DI f32x4 mfma(bf16x8 a, bf16x8 b, f32x4 c) { return __builtin_amdgcn_mfma_f32_16x16x32_bf16(a, b, c, 0, 0, 0); }
DI f32x16 mfma32(bf16x8 a, bf16x8 b, f32x16 c) { return __builtin_amdgcn_mfma_f32_32x32x16_bf16(a, b, c, 0, 0, 0); }
DI float rbf(float a) { return __builtin_bit_cast(float, pk_bf16(0.f, a)); }
DI float relu(float a) { int v = __builtin_bit_cast(int, a); return __builtin_bit_cast(float, v > 0 ? v : 0); }
DI unsigned glu2(float g0, float g1, float u0, float u1, float nz) {
  return pk_bf16(rbf(relu(g0)) * rbf(u0), __builtin_fmaf(rbf(relu(g1)), rbf(u1), nz));
}
DI u64 next_tag(u64 v) {
  const u64 g = ((u64)(unsigned)__builtin_amdgcn_readfirstlane((unsigned)(v >> 32)) << 32) | (unsigned)__builtin_amdgcn_readfirstlane((unsigned)v);
  return g * 0x9E3779B97F4A7C15ull + 0xD1B54A32D192ED03ull;
}

__global__ __launch_bounds__(512, 2) void reglu_fused(const void* x, const void* wg, const void* wu, const void* wd, void* ws, void* out) {
  __shared__ __attribute__((aligned(256))) char smem_[10 * SL];
  LAS char* smem = (LAS char*)smem_;
  const int lbase = (int)(size_t)smem;
  SCHED();
  const int b = blockIdx.x, xcd = b & 7, idx = b >> 3;
  const int lane = threadIdx.x & 63;
  int w = __builtin_amdgcn_readfirstlane(threadIdx.x >> 6) ^ 4;
  u64 pc0; asm volatile("s_getpc_b64 %0\nreglu_code_begin:" : "=s"(pc0));
  const int mb = (xcd & 3) * 4 + (idx & 3), jh = xcd >> 2;
  int jl = idx >> 2;

  int vx[4], vw[4];
  auto setupx = [&](int l) {
    #pragma unroll
    for (int i = 0; i < 4; i++) {
      vx[i] = (mb * 128 + 32 * (w & 3) + 8 * i + (l >> 3)) * 1024 - 1024 * i + (((l & 7) ^ ((l >> 4) + 4 * (i & 1))) << 4);
      asm volatile("" : "+v"(vx[i]));
    }
  };
  auto setupw = [&](int l) {
    #pragma unroll
    for (int i = 0; i < 4; i++) {
      vw[i] = (32 * (w & 1) + 8 * i + (l >> 3)) * (F_ * 2) - 1024 * i + (jh * 7 + jl) * 256 + (((l & 7) ^ ((((l >> 4) & 1) | ((i & 1) << 1)) << 1)) << 4);
      asm volatile("" : "+v"(vw[i]));
    }
  };
  setupx(lane); setupw(lane);
  const int kx = ((idx + jl) & 1) ^ (((xcd ^ jh) & 1) << 2);
  const int lb = lbase + (w & 3) * 4096;
  u32x4 rx, rw;
  auto LX = [&](int t) { dma4(rx, lb + XSL[t] * SL, vx, (t ^ kx) * 128); };
  auto LA = [&](int t) { dma4(rw, lb + ASL[t] * SL, vw, (t ^ kx) * 64 * (F_ * 2)); };
  auto LB = [&](int t) { dma4(rw, lb + BSL[t] * SL, vw, (t ^ kx) * 64 * (F_ * 2) + 128); };
  if (__builtin_expect(w >= 4 && jl < 7, 1)) {
    int l0 = lb + XSL[0] * SL, s0 = kx * 128; unsigned c2 = S_ * D_ * 2, c3 = RSRC3;
    asm volatile("" : "+s"(l0), "+s"(s0), "+s"(c2), "+s"(c3));
    SCHED();
    rx = u32x4{(unsigned)(u64)x, (unsigned)((u64)x >> 32), c2, c3};
    dma4(rx, l0, vx, s0);
    SCHED();
    rw = mkrsrc((w & 2) ? wu : wg, D_ * F_ * 2);
    __builtin_amdgcn_s_setprio(3);
    LA(0); LB(0); LX(1); LA(1); WAITV(8); BAR();
    LB(1); LX(2); LA(2); LB(2); WAITV(12); BAR();
    LX(3); LA(3); LB(3); LX(4); WAITV(16); BAR();
    LA(4); LX(5); LA(5); WAITV(16); BAR();
    LX(6); LA(6); LX(7); WAITV(20); BAR();
    LA(7); LB(4);
  }
  asm volatile("" : "+s"(w), "+s"(jl));

  const bool prod = jl < 7;
  const int fb = jh * 7 + jl, me = jh * 8 + jl;
  const int ni = (jl + 4 - (int)((size_t)wd >> 7)) & 7;
  auto rh = __builtin_amdgcn_make_buffer_rsrc(ws, 0, S_ * F_ * 2, RSRC3);
  gu64* ctrl = (gu64*)((GAS char*)ws + CTRL_OFF + mb * 8192);
  gu64* const myfl = ctrl + (me >> 2) * 16;
  const int kh = (w & 3) >> 1, mh = w & 1;
  int rot = xcd == 7 ? 0 : xcd;
  auto pfb = [&](int kh_, int i) { int t = i + rot; return kh_ * 7 + (t >= 7 ? t - 7 : t); };
  LAS unsigned* const cnt = (LAS unsigned*)(smem + WS[13] * SL);

  int ln, ad[2], hso[7];
  f32x16 acc[2];
  auto setup2 = [&]() {
    ln = __builtin_amdgcn_mbcnt_hi(~0u, __builtin_amdgcn_mbcnt_lo(~0u, 0u)); asm volatile("" : "+v"(ln));
    #pragma unroll
    for (int nt = 0; nt < 2; nt++) ad[nt] = kh * 8192 + 128 * (8 * (ln >> 5) + ((ln & 15) >> 2)) + 64 * (nt ^ ((ln >> 3) & 1)) + 32 * ((ln >> 4) & 1) + 8 * (ln & 3);
  };
  auto setuph = [&](int half) {
    #pragma unroll
    for (int i = 0; i < 7; i++) { hso[i] = ((mb * 4 + jh * 2 + mh) * 112 + pfb(kh, i) * 8 + half * 4) * 1024; asm volatile("" : "+s"(hso[i])); }
  };
  auto rdws = [&](int slot, int un, int nt) { return trpair(smem + slot * SL + ad[nt], 2048 * un, 2048 * un + 512); };
  auto poll = [&](int set, u64 tag) {
    unsigned spins = 0;
    for (;;) {
      u64 v = tag;
      if (ln < 8) { u64 z = 0; asm volatile("global_atomic_or_x2 %0, %1, %2, off sc0 sc1\n\ts_waitcnt vmcnt(0)" : "=&v"(v) : "v"(myfl + set * 256 + kh * 8 + ln), "v"(z) : "memory"); }
      if (__all(v == tag)) break;
      if (++spins > (1u << 20)) break;
      __builtin_amdgcn_s_sleep(4);
    }
  };
  auto gemm2 = [&](auto L_, auto& wa, auto&& mid) {
    constexpr bool LD = decltype(L_)::value;
    u32x4 hf[28]; bf16x8 wb[28][2];
    auto ldh = [&](int j) { hf[j] = __builtin_amdgcn_raw_buffer_load_b128(rh, ln * 16 + (j & 3) * 1024, hso[j >> 2], AUX_SC1); };
    auto rdw = [&](int j) { if (j >= 16) { wb[j][0] = rdws(WS[(LD ? 0 : 7) + (j >> 2)], j & 3, 0); wb[j][1] = rdws(WS[(LD ? 0 : 7) + (j >> 2)], j & 3, 1); } };
    #pragma unroll
    for (int j = 0; j < HW; j++) ldh(j);
    SCHED();
    mid();
    #pragma unroll
    for (int i = 0; i < 16; i++) { acc[0][i] = 0.f; acc[1][i] = 0.f; }
    SCHED();
    #pragma unroll
    for (int j = 0; j < 28; j++) {
      if (!LD && j == XJ) { BAR(); SCHED(); }
      if (j + 2 < 28) rdw(j + 2);
      #pragma unroll
      for (int nt = 1; nt >= 0; nt--) acc[nt] = mfma32(j < 16 ? wa[j < 16 ? j : 0][nt] : wb[j][nt], __builtin_bit_cast(bf16x8, hf[j]), acc[nt]);
      if (j + HW < 28) ldh(j + HW);
      SGB(0x8, 1); SGB(0x20, 1); SGB(0x100, 1); SGB(0x8, 1); SGB(0x100, 3);
      SCHED();
    }
  };
  auto putp = [&](int slot) {
    LAS char* pw = smem + slot * SL + (mh * 32 + (ln & 31)) * 256;
    #pragma unroll
    for (int nt = 0; nt < 2; nt++)
      #pragma unroll
      for (int i = 0; i < 4; i++)
        *(LAS f32x4*)(pw + 16 * ((8 * nt + 2 * i + (ln >> 5)) ^ (ln & 7))) = f32x4{acc[nt][4 * i], acc[nt][4 * i + 1], acc[nt][4 * i + 2], acc[nt][4 * i + 3]};
  };
  auto getp = [&](int slot, int c) { return *(LAS f32x4*)(smem + slot * SL + (8 * w + (ln >> 3)) * 256 + 16 * ((2 * (ln & 7) + c) ^ (ln >> 3))); };
  auto finish = [&]() {
    const f32x4 a = (getp(WS[4], 0) + getp(WS[5], 0)) + (getp(WS[6], 0) + getp(WS[7], 0));
    const f32x4 c = (getp(WS[4], 1) + getp(WS[5], 1)) + (getp(WS[6], 1) + getp(WS[7], 1));
    const u32x4 o = {pk_bf16(a[0], a[1]), pk_bf16(a[2], a[3]), pk_bf16(c[0], c[1]), pk_bf16(c[2], c[3])};
    auto ro = __builtin_amdgcn_make_buffer_rsrc(out, 0, S_ * D_ * 2, RSRC3);
    __builtin_amdgcn_raw_buffer_store_b128(o, ro, (mb * 128 + jh * 64 + 8 * w + (ln >> 3)) * 1024 + ni * 128 + (ln & 7) * 16, 0, AUX_SC1);
  };

  if (w >= 4) {
    rw = mkrsrc((w & 2) ? wu : wg, D_ * F_ * 2);
    setup2(); setupw(ln);
    const u32x4 rwd = mkrsrc(wd, F_ * D_ * 2);
    int vd[4], lbw = lb;
    #pragma unroll
    for (int i = 0; i < 4; i++) vd[i] = (32 * mh + 8 * i + (ln >> 3)) * 1024 - 1024 * i + ni * 128 + (((ln & 7) ^ (((ln >> 4) & 1) << 2)) << 4);
    auto LWs = [&](int i, int slot) { dma4(rwd, lbw + slot * SL, vd, (i < 7 ? pfb(kh, i) * 128 : pfb(kh, i - 7) * 128 + 64) * 1024); };
    auto LW = [&](int i) { LWs(i, WS[i]); };
    bf16x8 wa[16][2];
    auto rdR = [&](int k, int slot) {
      #pragma unroll
      for (int j = 4 * k; j < 4 * k + 4; j++) { wa[j][0] = rdws(slot, j & 3, 0); wa[j][1] = rdws(slot, j & 3, 1); }
    };
    u64 tag;
    __builtin_amdgcn_s_setprio(3);
    if (prod) {
      u64 gen;
      LW(0); WAITV(24); BAR();
      asm volatile("global_load_dwordx2 %0, %1, off sc1" : "=v"(gen) : "v"(ctrl + 512) : "memory");
      LW(1); WAITV(21); BAR();
      LW(2); WAITV(17); BAR();
      LB(5); asm volatile("s_waitcnt vmcnt(4)" : "+v"(gen) :: "memory"); BAR();
      tag = next_tag(gen);
      LB(6); rdR(0, WS[0]); rdR(1, WS[1]); rdR(2, WS[2]); WAITL(0); SCHED(); BAR();
      LB(7); LW(3); LW(4); WAITV(16); BAR();
      LW(5); LW(6); WAITV(12); BAR();
      LW(7); LW(8); rdR(3, WS[3]);
      if (w == 4 && ln < RN) __hip_atomic_store(ctrl + ln * 16 + me, tag, RLX_AGENT);
      WAITL(0); SCHED(); BAR();
      LW(9); LW(10);
      if (w == 5) *cnt = 0u;
      WAITL(0); SCHED(); BAR();
      LW(11);
    } else {
      tag = next_tag(__hip_atomic_load(ctrl + 512, RLX_AGENT));
      asm volatile("" : "+s"(tag));
      BAR();
      LWs(0, WS[9]); LWs(1, WS[10]); LWs(2, WS[11]); LWs(3, WS[12]); LW(4); LW(5); LW(6); LW(7); LW(8);
      WAITV(20); BAR();
      rdR(0, WS[9]); rdR(1, WS[10]); rdR(2, WS[11]); rdR(3, WS[12]);
      WAITL(0); SCHED(); BAR();
      LW(9); LW(10); LW(11);
    }
    asm volatile("" : "+s"(lbw), "+s"(rot));
    __builtin_amdgcn_s_setprio(0);
    setuph(0);
    u64 pa = tag;
    if (ln < 8) { u64 z = 0; pa = ~tag; asm volatile("global_atomic_or_x2 %0, %1, %2, off sc0 sc1\n\ts_nop 1" : "+v"(pa) : "v"(myfl + kh * 8 + ln), "v"(z) : "memory"); }
    LW(12);
    asm volatile("s_waitcnt vmcnt(4)" : "+v"(pa) :: "memory");
    if (!__all(pa == tag)) poll(0, tag);
    __builtin_amdgcn_fence(__ATOMIC_ACQUIRE, "wavefront");
    gemm2(std::true_type{}, wa, [&]() {
      asm volatile("s_waitcnt vmcnt(%0)" :: "n"(HW) : "memory");
      BAR();
      LW(13); });
    WAITV(0); WAITL(0); BAR();
    putp(kh ? WS[5] : WS[4]);
    if (w == 4 && me == 0 && ln == 0) __hip_atomic_store(ctrl + 512, tag, RLX_AGENT);
    WAITL(0); BAR();
    finish();
  } else {
    u64 tag;
    if (prod) {
      u64 gen;
      const int g = lane >> 4, q = (lane & 15) >> 2, p = lane & 3, row = lane & 15;
      int awb[3], axb[2][3];
      #pragma unroll
      for (int k = 0; k < 3; k++) {
        awb[k] = lbase + k * 65536 + 128 * (8 * g + q) + 32 * (w ^ ((q >> 1) | ((g & 1) << 1))) + 8 * p;
        axb[0][k] = lbase + k * 65536 + 128 * row + 16 * (g ^ ((row >> 1) & 7));
        axb[1][k] = lbase + k * 65536 + 128 * row + 16 * ((4 + g) ^ ((row >> 1) & 7));
        asm volatile("" : "+v"(awb[k]), "+v"(axb[0][k]), "+v"(axb[1][k]));
      }
      f32x4 ag[2][8], au[2][8];
      #pragma unroll
      for (int c = 0; c < 2; c++) for (int m = 0; m < 8; m++) { ag[c][m] = f32x4{0, 0, 0, 0}; au[c][m] = f32x4{0, 0, 0, 0}; }
      bf16x8 fg[2][2], fu[2][2], fx[2][8];
      auto rd = [&](int st, int s) {
        if (as_(st) >= 0) { LAS char* pb = (LAS char*)(size_t)awb[as_(st) >> 2]; const int o = (as_(st) & 3) * SL + 4096 * s; fg[s][0] = trpair(pb, o, o + 512); fu[s][0] = trpair(pb, o + 8192, o + 8192 + 512); }
        if (bs_(st) >= 0) { LAS char* pb = (LAS char*)(size_t)awb[bs_(st) >> 2]; const int o = (bs_(st) & 3) * SL + 4096 * s; fg[s][1] = trpair(pb, o, o + 512); fu[s][1] = trpair(pb, o + 8192, o + 8192 + 512); }
        #pragma unroll
        for (int m = 0; m < 8; m++) fx[s][m] = *(LAS bf16x8*)((LAS char*)(size_t)axb[s][xs_(st) >> 2] + (xs_(st) & 3) * SL + m * 2048);
      };
      auto mm1 = [&](int s, int c, int m) { ag[c][m] = mfma(fg[s][c], fx[s][m], ag[c][m]); au[c][m] = mfma(fu[s][c], fx[s][m], au[c][m]); };
      auto mm = [&](int st, int s) {
        #pragma unroll
        for (int m = 0; m < 8; m++) { if (as_(st) >= 0) mm1(s, 0, m); if (bs_(st) >= 0) mm1(s, 1, m); }
      };
      auto shape = [&](int nm, int nd) {
        if (nm == 32) {
          #pragma unroll
          for (int i = 0; i < nd - 8; i++) { SGB(0x8, 1); SGB(0x100, 1); }
          #pragma unroll
          for (int i = 0; i < 8; i++) { SGB(0x8, 2); SGB(0x100, 1); }
          SGB(0x8, 16);
        } else {
          #pragma unroll
          for (int i = 0; i < 16; i++) { SGB(0x8, 1); if (i < nd) SGB(0x100, 1); }
        }
      };
      int vst = (g >> 1) * 512 + (g & 1) * 256 + row * 16 + (mb * 448 + fb * 8 + w) * 1024;
      asm volatile("" : "+v"(vst));
      float nz = -0.0f; asm volatile("" : "+s"(nz));
      auto epi = [&](int c, int mp) {
        const int m = 2 * mp;
        unsigned a0 = glu2(ag[c][m][0], ag[c][m][1], au[c][m][0], au[c][m][1], nz);
        unsigned a1 = glu2(ag[c][m][2], ag[c][m][3], au[c][m][2], au[c][m][3], nz);
        unsigned c0 = glu2(ag[c][m + 1][0], ag[c][m + 1][1], au[c][m + 1][0], au[c][m + 1][1], nz);
        unsigned c1 = glu2(ag[c][m + 1][2], ag[c][m + 1][3], au[c][m + 1][2], au[c][m + 1][3], nz);
        auto r0 = __builtin_amdgcn_permlane16_swap(a0, c0, false, false);
        auto r1 = __builtin_amdgcn_permlane16_swap(a1, c1, false, false);
        __builtin_amdgcn_raw_buffer_store_b128(u32x4{r0[0], r1[0], r0[1], r1[1]}, rh, vst + (mp * 112 + c * 4) * 1024, 0, AUX_SC1);
      };
      #pragma unroll
      for (int m = 0; m < 8; m++) asm volatile("" : "+v"(ag[0][m]), "+v"(ag[1][m]), "+v"(au[0][m]), "+v"(au[1][m]));
      BAR();
      rd(0, 0);
      #pragma unroll
      for (int st = 0; st < 11; st++) {
        const int nm = st < 4 ? 32 : 16;
        SCHED();
        rd(st, 1); mm(st, 0); shape(nm, st < 4 ? 16 : 12);
        SCHED();
        if (st == 7) { asm volatile("s_waitcnt vmcnt(0)" : "+v"(gen) :: "memory"); tag = next_tag(gen); }
        if (st == 4) asm volatile("global_load_dwordx2 %0, %1, %2 sc1" : "=v"(gen) : "v"(0), "s"(ctrl + 512) : "memory");
        if (st == 9) WAITV(0);
        WAITL(0); BAR();
        SCHED();
        rd(st + 1, 0);
        if (st == 7) {
          #pragma unroll
          for (int m = 0; m < 4; m++) mm1(1, 0, m);
          #pragma unroll
          for (int i = 0; i < 8; i++) { SGB(0x8, 1); SGB(0x100, 1); if (i < 4) SGB(0x100, 1); }
          SCHED();
          #pragma unroll
          for (int m = 4; m < 8; m++) mm1(1, 0, m);
          epi(0, 0); epi(0, 1);
          #pragma unroll
          for (int i = 0; i < 8; i++) { SGB(0x8, 1); SGB(0x2, 9); }
          SCHED();
          epi(0, 2); epi(0, 3);
          SCHED();
          BAR();
        } else { mm(st, 1); shape(nm, st + 1 < 4 ? 16 : 12); }
        SCHED();
      }
      rd(11, 1);
      #pragma unroll
      for (int m = 0; m < 6; m++) mm1(0, 1, m);
      #pragma unroll
      for (int i = 0; i < 12; i++) { SGB(0x8, 1); SGB(0x100, 1); }
      SCHED();
      mm1(1, 1, 0); mm1(1, 1, 1); SCHED();
      WAITL(0); BAR(); SCHED();
      #pragma unroll
      for (int mp = 0; mp < 4; mp++) {
        if (mp < 3) { if (mp < 2) mm1(0, 1, 6 + mp); mm1(1, 1, 2 * mp + 2); mm1(1, 1, 2 * mp + 3); }
        epi(1, mp);
        if (mp < 2) {
          #pragma unroll
          for (int i = 0; i < 6; i++) { SGB(0x8, 1); SGB(0x2, 6); }
        } else if (mp < 3) {
          #pragma unroll
          for (int i = 0; i < 4; i++) { SGB(0x8, 1); SGB(0x2, 9); }
        }
        SCHED();
      }
    } else {
      const u64 gen = __hip_atomic_load(ctrl + 512, RLX_AGENT);
      unsigned keep[TB + 1];
      { unsigned csz; asm volatile("s_mov_b32 %0, reglu_code_end-reglu_code_begin" : "=s"(csz));
        auto rc = __builtin_amdgcn_make_buffer_rsrc((void*)pc0, 0, csz, RSRC3);
        keep[TB] = __builtin_amdgcn_raw_buffer_load_b32(rc, lane * 128 + w * 8192, 0, 0); }
      auto rr = __builtin_amdgcn_make_buffer_rsrc((void*)wd, 0, F_ * D_ * 2, RSRC3);
      const int n = ((idx & 3) * 4 + w) * 64 + lane;
      #pragma unroll
      for (int i = 0; i < TB; i++) keep[i] = __builtin_amdgcn_raw_buffer_load_b32(rr, (n & 511) * 128 + (n >> 9) * (7 * 131072), (pfb(0, i < 7 ? i : i - 7) * 128 + (i < 7 ? 0 : 64)) * 1024, 0);
      SCHED();
      tag = next_tag(gen);
      asm volatile("" : "+s"(tag));
      BAR();
      if (w == 0 && lane < 32 && (lane & 15) < RN) __hip_atomic_store(ctrl + lane * 16 + me, tag, RLX_AGENT);
      BAR(); BAR();
      #pragma unroll
      for (int i = 0; i <= TB; i++) asm volatile("" :: "v"(keep[i]));
    }
    setup2(); setuph(1);
    WAITV(0);
    if (prod) {
      unsigned old = 0;
      if (ln == 0) old = __hip_atomic_fetch_add(cnt, 1u, __ATOMIC_RELAXED, __HIP_MEMORY_SCOPE_WORKGROUP);
      if (__builtin_amdgcn_readfirstlane(old) == 3 && ln < RN) __hip_atomic_store(ctrl + 256 + ln * 16 + me, tag, RLX_AGENT);
    }
    BAR();
    bf16x8 wa[CNR][2];
    auto rdC = [&](int j0, int j1) {
      #pragma unroll
      for (int j = j0; j < j1; j++) { wa[j][0] = rdws(WS[7 + (j >> 2)], j & 3, 0); wa[j][1] = rdws(WS[7 + (j >> 2)], j & 3, 1); }
    };
    rdC(0, CN1);
    SCHED();
    __builtin_amdgcn_s_sleep(4);
    poll(1, tag);
    __builtin_amdgcn_fence(__ATOMIC_ACQUIRE, "wavefront");
    gemm2(std::false_type{}, wa, [&]() { rdC(CN1, CNR); });
    putp(kh ? WS[7] : WS[6]);
    WAITL(0); BAR();
    finish();
  }
  asm volatile("reglu_code_end:");
}

extern "C" void kernel_launch(void* const* d_in, const int* in_sizes, int n_in, void* d_out, int out_size,
                              void* d_ws, size_t ws_size, hipStream_t stream) {
  reglu_fused<<<256, 512, 0, stream>>>(d_in[0], d_in[1], d_in[2], d_in[3], d_ws, d_out);
}
```

```cpp
#include <hip/hip_runtime.h>
#include <hip/hip_bf16.h>
#include <type_traits>
#define LAS __attribute__((address_space(3)))
#define GAS __attribute__((address_space(1)))
typedef short s16x4 __attribute__((ext_vector_type(4)));
typedef short bf16x8 __attribute__((ext_vector_type(8)));
typedef float f32x2 __attribute__((ext_vector_type(2)));
typedef float f32x4 __attribute__((ext_vector_type(4)));
typedef float f32x16 __attribute__((ext_vector_type(16)));
typedef __bf16 bf16x2 __attribute__((ext_vector_type(2)));
typedef unsigned u32x4 __attribute__((ext_vector_type(4)));
typedef unsigned long long u64;
typedef GAS u64 gu64;
#define DI __device__ __forceinline__
#define RLX_AGENT __ATOMIC_RELAXED, __HIP_MEMORY_SCOPE_AGENT
#define WAITV(n) asm volatile("s_waitcnt vmcnt(" #n ")" ::: "memory")
#define WAITL(n) asm volatile("s_waitcnt lgkmcnt(" #n ")" ::: "memory")
#define BAR() do { asm volatile("" ::: "memory"); __builtin_amdgcn_s_barrier(); asm volatile("" ::: "memory"); } while (0)
#define SCHED() __builtin_amdgcn_sched_barrier(0)
#define SGB(m, n) __builtin_amdgcn_sched_group_barrier((m), (n), 0)

constexpr int S_ = 2048, D_ = 512, F_ = 1792;
constexpr int RSRC3 = 0x00020000, AUX_SC1 = 16;
constexpr size_t CTRL_OFF = 8u << 20;
constexpr int SL = 16384;
constexpr int RN = 16;
constexpr int HW = 9;
constexpr int TB = 10;
constexpr int CNR = 16, CN1 = 8;
constexpr int XJ = 4;
static_assert(XJ >= 4 && XJ <= 22 && CN1 >= 4, "barrier X placement");
constexpr int NF = 5, NS = 11, FAT = 9;
constexpr int XSL[8] = {0, 3, 6, 9, 2, 5, 7, 0}, ASL[8] = {1, 4, 7, 0, 3, 6, 9, 1}, BSL[8] = {2, 5, 8, 1, 4, 4, 8, 9};
constexpr int WS[14] = {8, 2, 3, 6, 1, 2, 3, 6, 5, 4, 7, 0, 9, 8};
constexpr int xs_(int st) { return XSL[st < 8 ? st : st - (8 - NF)]; }
constexpr int as_(int st) { return st < 8 ? ASL[st] : -1; }
constexpr int bs_(int st) { return st < NF ? BSL[st] : (st < 8 ? -1 : BSL[st - (8 - NF)]); }

DI unsigned pk_bf16(float a, float b) { f32x2 v = {a, b}; return __builtin_bit_cast(unsigned, __builtin_convertvector(v, bf16x2)); }
DI bf16x8 trpair(LAS char* p, int off0, int off1) {
  s16x4 a = __builtin_amdgcn_ds_read_tr16_b64_v4i16((LAS s16x4*)(p + off0));
  s16x4 b = __builtin_amdgcn_ds_read_tr16_b64_v4i16((LAS s16x4*)(p + off1));
  return __builtin_shufflevector(a, b, 0, 1, 2, 3, 4, 5, 6, 7);
}
DI u32x4 mkrsrc(const void* p, unsigned bytes) { u64 a = (u64)p; return u32x4{(unsigned)a, (unsigned)(a >> 32), bytes, (unsigned)RSRC3}; }
DI void dma4(u32x4 rs, int lds, const int (&v)[4], int soff) {
  asm volatile("s_mov_b32 m0, %0\n\ts_nop 0\n\t"
               "buffer_load_dwordx4 %1, %5, %6 offen lds\n\t"
               "buffer_load_dwordx4 %2, %5, %6 offen offset:1024 lds\n\t"
               "buffer_load_dwordx4 %3, %5, %6 offen offset:2048 lds\n\t"
               "buffer_load_dwordx4 %4, %5, %6 offen offset:3072 lds"
               :: "s"(lds), "v"(v[0]), "v"(v[1]), "v"(v[2]), "v"(v[3]), "s"(rs), "s"(soff) : "memory");
}
DI f32x4 mfma(bf16x8 a, bf16x8 b, f32x4 c) { return __builtin_amdgcn_mfma_f32_16x16x32_bf16(a, b, c, 0, 0, 0); }
DI f32x16 mfma32(bf16x8 a, bf16x8 b, f32x16 c) { return __builtin_amdgcn_mfma_f32_32x32x16_bf16(a, b, c, 0, 0, 0); }
DI float rbf(float a) { return __builtin_bit_cast(float, pk_bf16(0.f, a)); }
DI float relu(float a) { int v = __builtin_bit_cast(int, a); return __builtin_bit_cast(float, v > 0 ? v : 0); }
DI unsigned glu2(float g0, float g1, float u0, float u1, float nz) {
  return pk_bf16(rbf(relu(g0)) * rbf(u0), __builtin_fmaf(rbf(relu(g1)), rbf(u1), nz));
}
DI u64 next_tag(u64 v) {
  const u64 g = ((u64)(unsigned)__builtin_amdgcn_readfirstlane((unsigned)(v >> 32)) << 32) | (unsigned)__builtin_amdgcn_readfirstlane((unsigned)v);
  return g * 0x9E3779B97F4A7C15ull + 0xD1B54A32D192ED03ull;
}

__global__ __launch_bounds__(512, 2) void reglu_fused(const void* x, const void* wg, const void* wu, const void* wd, void* ws, void* out) {
  __shared__ __attribute__((aligned(256))) char smem_[10 * SL];
  LAS char* smem = (LAS char*)smem_;
  const int lbase = (int)(size_t)smem;
  SCHED();
  const int b = blockIdx.x, xcd = b & 7, idx = b >> 3;
  const int lane = threadIdx.x & 63;
  int w = __builtin_amdgcn_readfirstlane(threadIdx.x >> 6) ^ 4;
  u64 pc0; asm volatile("s_getpc_b64 %0\nreglu_code_begin:" : "=s"(pc0));
  const int mb = (xcd & 3) * 4 + (idx & 3), jh = xcd >> 2;
  int jl = idx >> 2;

  int vx[4], vw[4];
  auto setupx = [&](int l) {
    #pragma unroll
    for (int i = 0; i < 4; i++) {
      vx[i] = (mb * 128 + 32 * (w & 3) + 8 * i + (l >> 3)) * 1024 - 1024 * i + (((l & 7) ^ ((l >> 4) + 4 * (i & 1))) << 4);
      asm volatile("" : "+v"(vx[i]));
    }
  };
  auto setupw = [&](int l) {
    #pragma unroll
    for (int i = 0; i < 4; i++) {
      vw[i] = (32 * (w & 1) + 8 * i + (l >> 3)) * (F_ * 2) - 1024 * i + (jh * 7 + jl) * 256 + (((l & 7) ^ ((((l >> 4) & 1) | ((i & 1) << 1)) << 1)) << 4);
      asm volatile("" : "+v"(vw[i]));
    }
  };
  setupx(lane); setupw(lane);
  const int kx = ((idx + jl) & 1) ^ (((xcd ^ jh) & 1) << 2);
  const int lb = lbase + (w & 3) * 4096;
  u32x4 rx, rw;
  auto LX = [&](int t) { dma4(rx, lb + XSL[t] * SL, vx, (t ^ kx) * 128); };
  auto LA = [&](int t) { dma4(rw, lb + ASL[t] * SL, vw, (t ^ kx) * 64 * (F_ * 2)); };
  auto LB = [&](int t) { dma4(rw, lb + BSL[t] * SL, vw, (t ^ kx) * 64 * (F_ * 2) + 128); };
  if (__builtin_expect(w >= 4 && jl < 7, 1)) {
    int l0 = lb + XSL[0] * SL, s0 = kx * 128; unsigned c2 = S_ * D_ * 2, c3 = RSRC3;
    asm volatile("" : "+s"(l0), "+s"(s0), "+s"(c2), "+s"(c3));
    SCHED();
    rx = u32x4{(unsigned)(u64)x, (unsigned)((u64)x >> 32), c2, c3};
    dma4(rx, l0, vx, s0);
    SCHED();
    rw = mkrsrc((w & 2) ? wu : wg, D_ * F_ * 2);
    LA(0); LB(0); LX(1); LA(1); WAITV(8); BAR();
    LB(1); LX(2); LA(2); WAITV(8); BAR();
    LB(2); LX(3); LA(3); WAITV(8); BAR();
    LB(3); LX(4); LA(4); LB(4); WAITV(12); BAR();
    LX(5); LA(5); LX(6);
  }
  asm volatile("" : "+s"(w), "+s"(jl));

  const bool prod = jl < 7;
  const int fb = jh * 7 + jl, me = jh * 8 + jl;
  const int ni = (jl + 4 - (int)((size_t)wd >> 7)) & 7;
  auto rh = __builtin_amdgcn_make_buffer_rsrc(ws, 0, S_ * F_ * 2, RSRC3);
  gu64* ctrl = (gu64*)((GAS char*)ws + CTRL_OFF + mb * 8192);
  gu64* const myfl = ctrl + me * 16;
  const int kh = (w & 3) >> 1, mh = w & 1;
  int rot = xcd == 7 ? 0 : xcd;
  auto pfb = [&](int kh_, int i) { int t = i + rot; return kh_ * 7 + (t >= 7 ? t - 7 : t); };
  LAS unsigned* const cnt = (LAS unsigned*)(smem + WS[13] * SL);

  int ln, ad[2], hso[7];
  f32x16 acc[2];
  auto setup2 = [&]() {
    ln = __builtin_amdgcn_mbcnt_hi(~0u, __builtin_amdgcn_mbcnt_lo(~0u, 0u)); asm volatile("" : "+v"(ln));
    #pragma unroll
    for (int nt = 0; nt < 2; nt++) ad[nt] = kh * 8192 + 128 * (8 * (ln >> 5) + ((ln & 15) >> 2)) + 64 * (nt ^ ((ln >> 3) & 1)) + 32 * ((ln >> 4) & 1) + 8 * (ln & 3);
  };
  auto setuph = [&](int half) {
    #pragma unroll
    for (int i = 0; i < 7; i++) { hso[i] = ((mb * 4 + jh * 2 + mh) * 112 + pfb(kh, i) * 8 + half * 4) * 1024; asm volatile("" : "+s"(hso[i])); }
  };
  auto rdws = [&](int slot, int un, int nt) { return trpair(smem + slot * SL + ad[nt], 2048 * un, 2048 * un + 512); };
  auto poll = [&](int set, u64 tag) {
    unsigned spins = 0;
    for (;;) {
      u64 v = tag;
      if (ln < 8) { u64 z = 0; asm volatile("global_atomic_or_x2 %0, %1, %2, off sc0 sc1\n\ts_waitcnt vmcnt(0)" : "=&v"(v) : "v"(myfl + set * 256 + kh * 8 + ln), "v"(z) : "memory"); }
      if (__all(v == tag)) break;
      if (++spins > (1u << 20)) break;
      __builtin_amdgcn_s_sleep(4);
    }
  };
  auto gemm2 = [&](auto L_, auto& wa, auto&& mid) {
    constexpr bool LD = decltype(L_)::value;
    u32x4 hf[28]; bf16x8 wb[28][2];
    auto ldh = [&](int j) { hf[j] = __builtin_amdgcn_raw_buffer_load_b128(rh, ln * 16 + (j & 3) * 1024, hso[j >> 2], AUX_SC1); };
    auto rdw = [&](int j) { if (j >= 16) { wb[j][0] = rdws(WS[(LD ? 0 : 7) + (j >> 2)], j & 3, 0); wb[j][1] = rdws(WS[(LD ? 0 : 7) + (j >> 2)], j & 3, 1); } };
    #pragma unroll
    for (int j = 0; j < HW; j++) ldh(j);
    SCHED();
    mid();
    #pragma unroll
    for (int i = 0; i < 16; i++) { acc[0][i] = 0.f; acc[1][i] = 0.f; }
    SCHED();
    #pragma unroll
    for (int j = 0; j < 28; j++) {
      if (!LD && j == XJ) { BAR(); SCHED(); }
      if (j + 2 < 28) rdw(j + 2);
      #pragma unroll
      for (int nt = 1; nt >= 0; nt--) acc[nt] = mfma32(j < 16 ? wa[j < 16 ? j : 0][nt] : wb[j][nt], __builtin_bit_cast(bf16x8, hf[j]), acc[nt]);
      if (j + HW < 28) ldh(j + HW);
      SGB(0x8, 1); SGB(0x20, 1); SGB(0x100, 1); SGB(0x8, 1); SGB(0x100, 3);
      SCHED();
    }
  };
  auto putp = [&](int slot) {
    LAS char* pw = smem + slot * SL + (mh * 32 + (ln & 31)) * 256;
    #pragma unroll
    for (int nt = 0; nt < 2; nt++)
      #pragma unroll
      for (int i = 0; i < 4; i++)
        *(LAS f32x4*)(pw + 16 * ((8 * nt + 2 * i + (ln >> 5)) ^ (ln & 7))) = f32x4{acc[nt][4 * i], acc[nt][4 * i + 1], acc[nt][4 * i + 2], acc[nt][4 * i + 3]};
  };
  auto getp = [&](int slot, int c) { return *(LAS f32x4*)(smem + slot * SL + (8 * w + (ln >> 3)) * 256 + 16 * ((2 * (ln & 7) + c) ^ (ln >> 3))); };
  auto finish = [&]() {
    const f32x4 a = (getp(WS[4], 0) + getp(WS[5], 0)) + (getp(WS[6], 0) + getp(WS[7], 0));
    const f32x4 c = (getp(WS[4], 1) + getp(WS[5], 1)) + (getp(WS[6], 1) + getp(WS[7], 1));
    const u32x4 o = {pk_bf16(a[0], a[1]), pk_bf16(a[2], a[3]), pk_bf16(c[0], c[1]), pk_bf16(c[2], c[3])};
    auto ro = __builtin_amdgcn_make_buffer_rsrc(out, 0, S_ * D_ * 2, RSRC3);
    __builtin_amdgcn_raw_buffer_store_b128(o, ro, (mb * 128 + jh * 64 + 8 * w + (ln >> 3)) * 1024 + ni * 128 + (ln & 7) * 16, 0, AUX_SC1);
  };

  if (w >= 4) {
    rx = mkrsrc(x, S_ * D_ * 2); rw = mkrsrc((w & 2) ? wu : wg, D_ * F_ * 2);
    setup2(); setupw(ln); setupx(ln);
    const u32x4 rwd = mkrsrc(wd, F_ * D_ * 2);
    int vd[4], lbw = lb;
    #pragma unroll
    for (int i = 0; i < 4; i++) vd[i] = (32 * mh + 8 * i + (ln >> 3)) * 1024 - 1024 * i + ni * 128 + (((ln & 7) ^ (((ln >> 4) & 1) << 2)) << 4);
    auto LWs = [&](int i, int slot) { dma4(rwd, lbw + slot * SL, vd, (i < 7 ? pfb(kh, i) * 128 : pfb(kh, i - 7) * 128 + 64) * 1024); };
    auto LW = [&](int i) { LWs(i, WS[i]); };
    bf16x8 wa[16][2];
    auto rdR = [&](int k, int slot) {
      #pragma unroll
      for (int j = 4 * k; j < 4 * k + 4; j++) { wa[j][0] = rdws(slot, j & 3, 0); wa[j][1] = rdws(slot, j & 3, 1); }
    };
    u64 tag;
    if (prod) {
      u64 gen;
      auto GEN = [&]() { asm volatile("global_load_dwordx2 %0, %1, off sc1" : "=v"(gen) : "v"(ctrl + 512) : "memory"); };
      auto TAG = [&]() { asm volatile("" : "+v"(gen)); tag = next_tag(gen); };
      auto POSTA = [&]() { if (w == 4 && ln < RN) __hip_atomic_store(ctrl + ln * 16 + me, tag, RLX_AGENT); };
      auto CNT0 = [&]() { if (w == 5) *cnt = 0u; };
      LW(0); WAITV(16); BAR();
      GEN(); LA(6); LX(7); LA(7); WAITV(13); BAR();
      LW(1); LW(2); LB(5); rdR(0, WS[0]); WAITV(20); WAITL(0); SCHED(); BAR();
      LW(3); LB(6); WAITV(12); BAR();
      LB(7); rdR(1, WS[1]); rdR(2, WS[2]); WAITV(8); WAITL(0); SCHED(); BAR();
      TAG(); LW(4); LW(5); LW(6); rdR(3, WS[3]); WAITL(0); SCHED(); BAR();
      LW(7); WAITV(20); BAR();
      LW(8); LW(9); WAITV(24); BAR();
      LW(10); POSTA(); CNT0(); WAITL(0); SCHED(); BAR();
      LW(11);
    } else {
      tag = next_tag(__hip_atomic_load(ctrl + 512, RLX_AGENT));
      asm volatile("" : "+s"(tag));
      BAR();
      LWs(0, WS[9]); LWs(1, WS[10]); LWs(2, WS[11]); LWs(3, WS[12]); LW(4); LW(5); LW(6); LW(7); LW(8);
      WAITV(20); BAR();
      rdR(0, WS[9]); rdR(1, WS[10]); rdR(2, WS[11]); rdR(3, WS[12]);
      WAITL(0); SCHED(); BAR();
      LW(9); LW(10); LW(11);
    }
    asm volatile("" : "+s"(lbw), "+s"(rot));
    setuph(0);
    u64 pa = tag;
    if (ln < 8) { u64 z = 0; pa = ~tag; asm volatile("global_atomic_or_x2 %0, %1, %2, off sc0 sc1\n\ts_nop 1" : "+v"(pa) : "v"(myfl + kh * 8 + ln), "v"(z) : "memory"); }
    LW(12);
    asm volatile("s_waitcnt vmcnt(4)" : "+v"(pa) :: "memory");
    if (!__all(pa == tag)) poll(0, tag);
    __builtin_amdgcn_fence(__ATOMIC_ACQUIRE, "wavefront");
    gemm2(std::true_type{}, wa, [&]() {
      asm volatile("s_waitcnt vmcnt(%0)" :: "n"(HW) : "memory");
      BAR();
      LW(13); });
    WAITV(0); WAITL(0); BAR();
    putp(kh ? WS[5] : WS[4]);
    if (w == 4 && me == 0 && ln == 0) __hip_atomic_store(ctrl + 512, tag, RLX_AGENT);
    if (w == 5 && ln < 32) __hip_atomic_store(myfl + (ln >> 4) * 256 + (ln & 15), ~tag, RLX_AGENT);
    WAITL(0); BAR();
    finish();
  } else {
    u64 tag;
    if (prod) {
      u64 gen;
      const int g = lane >> 4, q = (lane & 15) >> 2, p = lane & 3, row = lane & 15;
      int awb[3], axb[2][3];
      #pragma unroll
      for (int k = 0; k < 3; k++) {
        awb[k] = lbase + k * 65536 + 128 * (8 * g + q) + 32 * (w ^ ((q >> 1) | ((g & 1) << 1))) + 8 * p;
        axb[0][k] = lbase + k * 65536 + 128 * row + 16 * (g ^ ((row >> 1) & 7));
        axb[1][k] = lbase + k * 65536 + 128 * row + 16 * ((4 + g) ^ ((row >> 1) & 7));
        asm volatile("" : "+v"(awb[k]), "+v"(axb[0][k]), "+v"(axb[1][k]));
      }
      f32x4 ag[2][8], au[2][8];
      #pragma unroll
      for (int c = 0; c < 2; c++) for (int m = 0; m < 8; m++) { ag[c][m] = f32x4{0, 0, 0, 0}; au[c][m] = f32x4{0, 0, 0, 0}; }
      bf16x8 fg[2][2], fu[2][2], fx[2][8];
      auto rd = [&](int st, int s) {
        if (as_(st) >= 0) { LAS char* pb = (LAS char*)(size_t)awb[as_(st) >> 2]; const int o = (as_(st) & 3) * SL + 4096 * s; fg[s][0] = trpair(pb, o, o + 512); fu[s][0] = trpair(pb, o + 8192, o + 8192 + 512); }
        if (bs_(st) >= 0) { LAS char* pb = (LAS char*)(size_t)awb[bs_(st) >> 2]; const int o = (bs_(st) & 3) * SL + 4096 * s; fg[s][1] = trpair(pb, o, o + 512); fu[s][1] = trpair(pb, o + 8192, o + 8192 + 512); }
        #pragma unroll
        for (int m = 0; m < 8; m++) fx[s][m] = *(LAS bf16x8*)((LAS char*)(size_t)axb[s][xs_(st) >> 2] + (xs_(st) & 3) * SL + m * 2048);
      };
      auto mm1 = [&](int s, int c, int m) {
        if (m & 1) { au[c][m] = mfma(fu[s][c], fx[s][m], au[c][m]); ag[c][m] = mfma(fg[s][c], fx[s][m], ag[c][m]); }
        else { ag[c][m] = mfma(fg[s][c], fx[s][m], ag[c][m]); au[c][m] = mfma(fu[s][c], fx[s][m], au[c][m]); }
      };
      auto mm = [&](int st, int s) {
        #pragma unroll
        for (int m = 0; m < 8; m++) {
          if (m & 1) { if (bs_(st) >= 0) mm1(s, 1, m); if (as_(st) >= 0) mm1(s, 0, m); }
          else { if (as_(st) >= 0) mm1(s, 0, m); if (bs_(st) >= 0) mm1(s, 1, m); }
        }
      };
      auto shape = [&](int nm, int nd) {
        if (nm == 32) {
          #pragma unroll
          for (int i = 0; i < nd - 8; i++) { SGB(0x8, 1); SGB(0x100, 1); }
          #pragma unroll
          for (int i = 0; i < 8; i++) { SGB(0x8, 2); SGB(0x100, 1); }
          SGB(0x8, 16);
        } else {
          #pragma unroll
          for (int i = 0; i < 16; i++) { SGB(0x8, 1); if (i < nd) SGB(0x100, 1); }
        }
      };
      int vst = (g >> 1) * 512 + (g & 1) * 256 + row * 16 + (mb * 448 + fb * 8 + w) * 1024;
      asm volatile("" : "+v"(vst));
      float nz = -0.0f; asm volatile("" : "+s"(nz));
      auto epi = [&](int c, int mp) {
        const int m = 2 * mp;
        unsigned a0 = glu2(ag[c][m][0], ag[c][m][1], au[c][m][0], au[c][m][1], nz);
        unsigned a1 = glu2(ag[c][m][2], ag[c][m][3], au[c][m][2], au[c][m][3], nz);
        unsigned c0 = glu2(ag[c][m + 1][0], ag[c][m + 1][1], au[c][m + 1][0], au[c][m + 1][1], nz);
        unsigned c1 = glu2(ag[c][m + 1][2], ag[c][m + 1][3], au[c][m + 1][2], au[c][m + 1][3], nz);
        auto r0 = __builtin_amdgcn_permlane16_swap(a0, c0, false, false);
        auto r1 = __builtin_amdgcn_permlane16_swap(a1, c1, false, false);
        __builtin_amdgcn_raw_buffer_store_b128(u32x4{r0[0], r1[0], r0[1], r1[1]}, rh, vst + (mp * 112 + c * 4) * 1024, 0, AUX_SC1);
      };
      #pragma unroll
      for (int m = 0; m < 8; m++) asm volatile("" : "+v"(ag[0][m]), "+v"(ag[1][m]), "+v"(au[0][m]), "+v"(au[1][m]));
      __builtin_amdgcn_s_setprio(3);
      BAR();
      rd(0, 0);
      #pragma unroll
      for (int st = 0; st < NS - 1; st++) {
        const int nm = st < NF ? 32 : 16;
        SCHED();
        rd(st, 1); mm(st, 0); shape(nm, st < NF ? 16 : 12);
        SCHED();
        if (st == 7) { asm volatile("s_waitcnt vmcnt(0)" : "+v"(gen) :: "memory"); tag = next_tag(gen); }
        if (st == 5) asm volatile("global_load_dwordx2 %0, %1, %2 sc1" : "=v"(gen) : "v"(0), "s"(ctrl + 512) : "memory");
        if (st == FAT) WAITV(0);
        WAITL(0); BAR();
        SCHED();
        rd(st + 1, 0);
        if (st == 7) {
          #pragma unroll
          for (int m = 0; m < 4; m++) mm1(1, 0, m);
          #pragma unroll
          for (int i = 0; i < 8; i++) { SGB(0x8, 1); SGB(0x100, 1); if (i < 4) SGB(0x100, 1); }
          SCHED();
          #pragma unroll
          for (int m = 4; m < 8; m++) mm1(1, 0, m);
          epi(0, 0); epi(0, 1);
          #pragma unroll
          for (int i = 0; i < 8; i++) { SGB(0x8, 1); SGB(0x2, 9); }
          SCHED();
          epi(0, 2); epi(0, 3);
          SCHED();
          BAR();
        } else { mm(st, 1); shape(nm, st + 1 < NF ? 16 : 12); }
        SCHED();
      }
      rd(NS - 1, 1);
      #pragma unroll
      for (int m = 0; m < 6; m++) mm1(0, 1, m);
      #pragma unroll
      for (int i = 0; i < 12; i++) { SGB(0x8, 1); SGB(0x100, 1); }
      SCHED();
      mm1(1, 1, 0); mm1(1, 1, 1); SCHED();
      WAITL(0); BAR(); SCHED();
      #pragma unroll
      for (int mp = 0; mp < 4; mp++) {
        if (mp < 3) { if (mp < 2) mm1(0, 1, 6 + mp); mm1(1, 1, 2 * mp + 2); mm1(1, 1, 2 * mp + 3); }
        epi(1, mp);
        if (mp < 2) {
          #pragma unroll
          for (int i = 0; i < 6; i++) { SGB(0x8, 1); SGB(0x2, 6); }
        } else if (mp < 3) {
          #pragma unroll
          for (int i = 0; i < 4; i++) { SGB(0x8, 1); SGB(0x2, 9); }
        }
        SCHED();
      }
    } else {
      const u64 gen = __hip_atomic_load(ctrl + 512, RLX_AGENT);
      unsigned keep[TB + 1];
      { unsigned csz; asm volatile("s_mov_b32 %0, reglu_code_end-reglu_code_begin" : "=s"(csz));
        auto rc = __builtin_amdgcn_make_buffer_rsrc((void*)pc0, 0, csz, RSRC3);
        keep[TB] = __builtin_amdgcn_raw_buffer_load_b32(rc, lane * 128 + w * 8192, 0, 0); }
      auto rr = __builtin_amdgcn_make_buffer_rsrc((void*)wd, 0, F_ * D_ * 2, RSRC3);
      const int n = ((idx & 3) * 4 + w) * 64 + lane;
      #pragma unroll
      for (int i = 0; i < TB; i++) keep[i] = __builtin_amdgcn_raw_buffer_load_b32(rr, (n & 511) * 128 + (n >> 9) * (7 * 131072), (pfb(0, i < 7 ? i : i - 7) * 128 + (i < 7 ? 0 : 64)) * 1024, 0);
      SCHED();
      tag = next_tag(gen);
      asm volatile("" : "+s"(tag));
      BAR();
      if (w == 0 && lane < 32 && (lane & 15) < RN) __hip_atomic_store(ctrl + lane * 16 + me, tag, RLX_AGENT);
      BAR(); BAR();
      #pragma unroll
      for (int i = 0; i <= TB; i++) asm volatile("" :: "v"(keep[i]));
    }
    __builtin_amdgcn_s_setprio(0);
    setup2(); setuph(1);
    WAITV(0);
    if (prod) {
      unsigned old = 0;
      if (ln == 0) old = __hip_atomic_fetch_add(cnt, 1u, __ATOMIC_RELAXED, __HIP_MEMORY_SCOPE_WORKGROUP);
      if (__builtin_amdgcn_readfirstlane(old) == 3 && ln < RN) __hip_atomic_store(ctrl + 256 + ln * 16 + me, tag, RLX_AGENT);
    }
    BAR();
    bf16x8 wa[CNR][2];
    auto rdC = [&](int j0, int j1) {
      #pragma unroll
      for (int j = j0; j < j1; j++) { wa[j][0] = rdws(WS[7 + (j >> 2)], j & 3, 0); wa[j][1] = rdws(WS[7 + (j >> 2)], j & 3, 1); }
    };
    rdC(0, CN1);
    SCHED();
    __builtin_amdgcn_s_sleep(1);
    poll(1, tag);
    __builtin_amdgcn_fence(__ATOMIC_ACQUIRE, "wavefront");
    gemm2(std::false_type{}, wa, [&]() { rdC(CN1, CNR); });
    putp(kh ? WS[7] : WS[6]);
    WAITL(0); BAR();
    finish();
  }
  asm volatile("reglu_code_end:");
}

extern "C" void kernel_launch(void* const* d_in, const int* in_sizes, int n_in, void* d_out, int out_size,
                              void* d_ws, size_t ws_size, hipStream_t stream) {
  reglu_fused<<<256, 512, 0, stream>>>(d_in[0], d_in[1], d_in[2], d_in[3], d_ws, d_out);
}
```
